```python
import torch

def _softmax(x, axis=-1):
    return torch.softmax(x, dim=axis)
def _gelu(x):
    return torch.nn.functional.gelu(x, approximate="tanh")
def _silu(x):
    return torch.nn.functional.silu(x)
def _sigmoid(x):
    return torch.sigmoid(x)
def _layernorm(x, axis=-1, eps=1e-5):
    m = x.mean(dim=axis, keepdim=True)
    v = x.var(dim=axis, keepdim=True, unbiased=False)
    return (x - m) / torch.sqrt(v + eps)
def _scatter_add(tgt, idx, src, axis=0):
    return tgt.clone().index_add_(axis, idx, src)
def _segment_sum(src, seg, n):
    out = torch.zeros((n,) + src.shape[1:], dtype=src.dtype)
    return out.index_add_(0, seg, src)
def _pad(x, pw):
    flat = tuple(v for pair in reversed(pw) for v in pair)
    return torch.nn.functional.pad(x, flat)

kernel_name = 'concat_heads__gqa_block_cached__sliding_block_cached__decode'

def setup_inputs():

    _w = (lambda a: (lambda t: t.to(torch.bfloat16) if t.is_floating_point() else t)(torch.as_tensor(a)))
    def _A_setup():
        B, Sq, D = 1, 1, 640
        Hq, Hkv, Sk, Dh = 32, 32, 8192, 128
        assert Hq % Hkv == 0, (Hq, Hkv)
        return {
            "x": _w((lambda *s: torch.randn(*s, dtype=torch.bfloat16) * 0.8)(B, Sq, D)),
            "k_cache": _w((lambda *s: torch.randn(*s, dtype=torch.bfloat16) * 1.6)(B, Hkv, Sk, Dh)),
            "v_cache": _w((lambda *s: torch.randn(*s, dtype=torch.bfloat16) * 0.2)(B, Hkv, Sk, Dh)),
            "wq": _w((lambda *s: torch.randn(*s, dtype=torch.bfloat16) * 1.6)(D, Hq * Dh) * (D ** -0.5)),
            "wo": _w((lambda *s: torch.randn(*s, dtype=torch.bfloat16) * 0.2)(Hq * Dh, D) * ((Hq * Dh) ** -0.5)),
        }
    _A_in = _A_setup()

    def _B_setup():
        B, Sq, D = 1, 1, 640
        Hq, Hkv, Sk, Dh = 32, 32, 8192, 128
        assert Hq % Hkv == 0, (Hq, Hkv)
        return {
            "x": _w((lambda *s: torch.randn(*s, dtype=torch.bfloat16) * 0.8)(B, Sq, D)),
            "k_cache": _w((lambda *s: torch.randn(*s, dtype=torch.bfloat16) * 1.6)(B, Hkv, Sk, Dh)),
            "v_cache": _w((lambda *s: torch.randn(*s, dtype=torch.bfloat16) * 0.2)(B, Hkv, Sk, Dh)),
            "wq": _w((lambda *s: torch.randn(*s, dtype=torch.bfloat16) * 1.6)(D, Hq * Dh) * (D ** -0.5)),
            "wo": _w((lambda *s: torch.randn(*s, dtype=torch.bfloat16) * 0.2)(Hq * Dh, D) * ((Hq * Dh) ** -0.5)),
        }
    _B_in = _B_setup()

    return {
        "A_x": _A_in["x"],
        "A_k_cache": _A_in["k_cache"],
        "A_v_cache": _A_in["v_cache"],
        "A_wq": _A_in["wq"],
        "A_wo": _A_in["wo"],
        "B_x": _B_in["x"],
        "B_k_cache": _B_in["k_cache"],
        "B_v_cache": _B_in["v_cache"],
        "B_wq": _B_in["wq"],
        "B_wo": _B_in["wo"],
    }

def reference(A_x, A_k_cache, A_v_cache, A_wq, A_wo, B_x, B_k_cache, B_v_cache, B_wq, B_wo):
    def _A_ref(x, k_cache, v_cache, wq, wo):
        B, Sq, D = x.shape
        Hq, Dh = 32, 128
        Hkv = k_cache.shape[1]
        rep = Hq // Hkv
        q = (lambda x, *d: x.permute(*d))((x @ wq).reshape(B, Sq, Hq, Dh), 0, 2, 1, 3)
        k = (lambda x, n, axis: torch.repeat_interleave(x, n, dim=axis))(k_cache, rep, axis=1)
        v = (lambda x, n, axis: torch.repeat_interleave(x, n, dim=axis))(v_cache, rep, axis=1)
        scores = torch.einsum("bhqd,bhkd->bhqk", q, k) * (Dh ** -0.5)
        p = _softmax(scores, axis=-1)
        o = torch.einsum("bhqk,bhkd->bhqd", p, v)
        return (lambda x, *d: x.permute(*d))(o, 0, 2, 1, 3).reshape(B, Sq, Hq * Dh) @ wo

    def _B_ref(x, k_cache, v_cache, wq, wo):
        B, Sq, D = x.shape
        Hq, Dh = 32, 128
        Hkv, Sk = k_cache.shape[1], k_cache.shape[2]
        W = min(4096, max(256, Sk // 4))
        rep = Hq // Hkv
        q = (lambda x, *d: x.permute(*d))((x @ wq).reshape(B, Sq, Hq, Dh), 0, 2, 1, 3)
        k = (lambda x, n, axis: torch.repeat_interleave(x, n, dim=axis))(k_cache[:, :, Sk - W:, :], rep, axis=1)
        v = (lambda x, n, axis: torch.repeat_interleave(x, n, dim=axis))(v_cache[:, :, Sk - W:, :], rep, axis=1)
        scores = torch.einsum("bhqd,bhkd->bhqk", q, k) * (Dh ** -0.5)
        p = _softmax(scores, axis=-1)
        o = torch.einsum("bhqk,bhkd->bhqd", p, v)
        return (lambda x, *d: x.permute(*d))(o, 0, 2, 1, 3).reshape(B, Sq, Hq * Dh) @ wo

    a = _A_ref(A_x, A_k_cache, A_v_cache, A_wq, A_wo)
    b = _B_ref(B_x, B_k_cache, B_v_cache, B_wq, B_wo)
    return torch.concatenate([a, b], axis=-1)
```

```cpp
#include <hip/hip_runtime.h>
#include <hip/hip_bf16.h>
#include <stdint.h>

typedef unsigned u32x4 __attribute__((ext_vector_type(4)));
typedef unsigned u32x2 __attribute__((ext_vector_type(2)));
typedef float f32x4 __attribute__((ext_vector_type(4)));
typedef __bf16 bf16x2 __attribute__((ext_vector_type(2)));

constexpr int DM = 640, NH = 32, DH = 128, SK = 8192, WIN = 2048;
constexpr int NWAVE = 8, NTHR = NWAVE * 64;
constexpr int SPLIT_A = 6, SPLIT_B = 2;
constexpr int NWG_A = NH * SPLIT_A, NWG_B = NH * SPLIT_B, NWG = NWG_A + NWG_B;
constexpr float SC2 = 0.088388347648318440f * 1.4426950408889634f;

#define SB() __builtin_amdgcn_sched_barrier(0)
#define PIN(x) asm volatile("" : "+v"(x))
#define PIN8(a) do { PIN(a[0]); PIN(a[1]); PIN(a[2]); PIN(a[3]); PIN(a[4]); PIN(a[5]); PIN(a[6]); PIN(a[7]); } while (0)
__device__ __forceinline__ float dot2(unsigned k, unsigned q, float acc) {
  return __builtin_amdgcn_fdot2_f32_bf16(__builtin_bit_cast(bf16x2, k), __builtin_bit_cast(bf16x2, q), acc, false);
}
template <int CTRL> __device__ __forceinline__ float dpp(float x) {
  return __builtin_bit_cast(float, __builtin_amdgcn_mov_dpp(__builtin_bit_cast(int, x), CTRL, 0xf, 0xf, true));
}
constexpr int XOR1 = 0xB1, XOR2 = 0x4E, XOR7 = 0x141, XOR8 = 0x128;
__device__ __forceinline__ float xrow_max(float x) {
  auto s = __builtin_amdgcn_permlane16_swap(__float_as_uint(x), __float_as_uint(x), false, false);
  x = fmaxf(__uint_as_float(s[0]), __uint_as_float(s[1]));
  auto t = __builtin_amdgcn_permlane32_swap(__float_as_uint(x), __float_as_uint(x), false, false);
  return fmaxf(__uint_as_float(t[0]), __uint_as_float(t[1]));
}
__device__ __forceinline__ float xrow_sum(float x) {
  auto s = __builtin_amdgcn_permlane16_swap(__float_as_uint(x), __float_as_uint(x), false, false);
  x = __uint_as_float(s[0]) + __uint_as_float(s[1]);
  auto t = __builtin_amdgcn_permlane32_swap(__float_as_uint(x), __float_as_uint(x), false, false);
  return __uint_as_float(t[0]) + __uint_as_float(t[1]);
}
__device__ __forceinline__ float wave_max(float x) {
  x = fmaxf(x, dpp<XOR1>(x)); x = fmaxf(x, dpp<XOR2>(x)); x = fmaxf(x, dpp<XOR7>(x)); x = fmaxf(x, dpp<XOR8>(x));
  return xrow_max(x);
}
__device__ __forceinline__ float wave_sum(float x) {
  x += dpp<XOR1>(x); x += dpp<XOR2>(x); x += dpp<XOR7>(x); x += dpp<XOR8>(x);
  return xrow_sum(x);
}
__device__ __forceinline__ float bf_lo(unsigned u) { return __uint_as_float(u << 16); }
__device__ __forceinline__ float bf_hi(unsigned u) { return __uint_as_float(u & 0xffff0000u); }
__device__ __forceinline__ unsigned pack_bf16(float lo, float hi) {
  unsigned r; asm("v_cvt_pk_bf16_f32 %0, %1, %2" : "=v"(r) : "v"(lo), "v"(hi)); return r;
}
__device__ __forceinline__ u32x4 ldnt(const u32x4* p) { return __builtin_nontemporal_load(p); }

__device__ __forceinline__ float transpose_reduce16(float (&x)[16], int c) {
#define TR_STEP(HALF, CTRL, BIT)                                                          \
  _Pragma("unroll") for (int h = 0; h < (HALF); ++h) {                                    \
    const float lo_ = x[h], hi_ = x[h + (HALF)];                                          \
    const float t1_ = lo_ + dpp<CTRL>(lo_), t2_ = hi_ + dpp<CTRL>(hi_);                   \
    x[h] = (c & (BIT)) ? t2_ : t1_; }
  TR_STEP(8, XOR8, 8) TR_STEP(4, XOR7, 4) TR_STEP(2, XOR2, 2) TR_STEP(1, XOR1, 1)
#undef TR_STEP
  return x[0];
}

template <int CNT>
__device__ __forceinline__ void issue(u32x4 (&buf)[16], const u32x4* p, int base, int last) {
#pragma unroll
  for (int t = 0; t < CNT; ++t) { const int s = min(base + t, last); buf[t] = ldnt(p + (size_t)s * 64); }
}
template <int CNT>
__device__ __forceinline__ float scores(const u32x4 (&buf)[16], const u32x4 q, int c) {
  float x[16];
#pragma unroll
  for (int t = 0; t < 16; ++t) {
    float d = 0.f;
    if (t < CNT) {
#pragma unroll
      for (int k = 0; k < 4; ++k) d = dot2(buf[t][k], q[k], d);
    }
    x[t] = d;
  }
  return transpose_reduce16(x, c);
}
template <int T> struct Bc { static __device__ __forceinline__ float get(float p) { return dpp<0x150 + T>(p); } };
template <int CNT, int T = 0>
__device__ __forceinline__ void pv(const u32x4 (&buf)[16], float p, float (&acc)[8]) {
  if constexpr (T < CNT) {
    const float pt = Bc<T>::get(p);
#pragma unroll
    for (int k = 0; k < 4; ++k) {
      acc[2 * k] = fmaf(pt, bf_lo(buf[T][k]), acc[2 * k]);
      acc[2 * k + 1] = fmaf(pt, bf_hi(buf[T][k]), acc[2 * k + 1]);
    }
    pv<CNT, T + 1>(buf, p, acc);
  }
}

struct __attribute__((aligned(16))) Lds {
  float qp[NWAVE][DH];
  float ap[NWAVE][DH];
  float ml[NWAVE][2];
  float o[DH];
  float yp[NWAVE][DM];
};

template <int NG3>
__device__ __forceinline__ void body(const __hip_bfloat16* __restrict__ x, const __hip_bfloat16* __restrict__ kc,
                                     const __hip_bfloat16* __restrict__ vc, const __hip_bfloat16* __restrict__ wq,
                                     const __hip_bfloat16* __restrict__ wo, int h, int slot0, int n, int row_off,
                                     float* __restrict__ y_out, float* __restrict__ ml_out, Lds& lds) {
  const int tid = threadIdx.x, lane = tid & 63, w = __builtin_amdgcn_readfirstlane(tid >> 6);
  const int r = lane >> 4, c = lane & 15;
  const int last = n - 1;
  const u32x4* kp = reinterpret_cast<const u32x4*>(kc + ((size_t)h * SK + row_off + 4 * (size_t)slot0) * DH) + lane;
  const u32x4* vp = reinterpret_cast<const u32x4*>(vc + ((size_t)h * SK + row_off + 4 * (size_t)slot0) * DH) + lane;

  const int d0 = 80 * w + 20 * r;
  u32x2 xr[5];
#pragma unroll
  for (int i = 0; i < 5; ++i) xr[i] = *reinterpret_cast<const u32x2*>(x + d0 + 4 * i);
  u32x4 wr[20];
#pragma unroll
  for (int i = 0; i < 20; ++i)
    wr[i] = *reinterpret_cast<const u32x4*>(wq + (size_t)(d0 + i) * (NH * DH) + h * DH + 8 * c);

  u32x4 bA[16], bB[16];
  issue<16>(bA, kp, 0, last);
  issue<16>(bB, kp, 16, last);
  SB();
  {
    float qa[8];
#pragma unroll
    for (int k = 0; k < 8; ++k) qa[k] = 0.f;
#pragma unroll
    for (int i = 0; i < 20; ++i) {
      const unsigned xw = xr[i >> 2][(i >> 1) & 1];
      const float xv = (i & 1) ? bf_hi(xw) : bf_lo(xw);
#pragma unroll
      for (int k = 0; k < 4; ++k) {
        qa[2 * k] = fmaf(xv, bf_lo(wr[i][k]), qa[2 * k]);
        qa[2 * k + 1] = fmaf(xv, bf_hi(wr[i][k]), qa[2 * k + 1]);
      }
    }
#pragma unroll
    for (int k = 0; k < 8; ++k) qa[k] = xrow_sum(qa[k]);
    if (r == 0) {
      *reinterpret_cast<f32x4*>(&lds.qp[w][8 * c]) = f32x4{qa[0], qa[1], qa[2], qa[3]};
      *reinterpret_cast<f32x4*>(&lds.qp[w][8 * c + 4]) = f32x4{qa[4], qa[5], qa[6], qa[7]};
    }
  }
  __syncthreads();
  u32x4 q;
  {
    f32x4 s0 = {0.f, 0.f, 0.f, 0.f}, s1 = {0.f, 0.f, 0.f, 0.f};
#pragma unroll
    for (int u = 0; u < NWAVE; ++u) {
      s0 += *reinterpret_cast<const f32x4*>(&lds.qp[u][8 * c]);
      s1 += *reinterpret_cast<const f32x4*>(&lds.qp[u][8 * c + 4]);
    }
    q = u32x4{pack_bf16(s0[0], s0[1]), pack_bf16(s0[2], s0[3]), pack_bf16(s1[0], s1[1]), pack_bf16(s1[2], s1[3])};
  }

  float s0, s1, s2 = -INFINITY;
  SB();
  s0 = scores<16>(bA, q, c) * SC2;
  PIN(s0); SB();
  if constexpr (NG3 > 0) issue<NG3>(bA, kp, 32, last); else issue<16>(bA, vp, 0, last);
  SB();
  s1 = scores<16>(bB, q, c) * SC2;
  PIN(s1); SB();
  if constexpr (NG3 > 0) {
    issue<16>(bB, vp, 0, last);
    SB();
    const float t = scores<NG3>(bA, q, c) * SC2;
    s2 = (32 + c < n) ? t : -INFINITY;
    PIN(s2); SB();
    issue<16>(bA, vp, 16, last);
  } else {
    issue<16>(bB, vp, 16, last);
  }
  SB();
  const float m = wave_max(fmaxf(fmaxf(s0, s1), s2));
  const float p0 = __builtin_amdgcn_exp2f(s0 - m), p1 = __builtin_amdgcn_exp2f(s1 - m);
  const float p2 = (NG3 > 0) ? __builtin_amdgcn_exp2f(s2 - m) : 0.f;
  const float l = wave_sum(p0 + p1 + p2);

  float acc[8];
#pragma unroll
  for (int k = 0; k < 8; ++k) acc[k] = 0.f;
  u32x4 wm[16], wt[4];
  const __hip_bfloat16* wop = wo + (size_t)(h * DH + 16 * w) * DM;
  if constexpr (NG3 > 0) {
    pv<16>(bB, p0, acc);
    PIN8(acc); SB();
    issue<NG3>(bB, vp, 32, last);
    SB();
    pv<16>(bA, p1, acc);
    PIN8(acc); SB();
#pragma unroll
    for (int i = 0; i < 16; ++i) wm[i] = *reinterpret_cast<const u32x4*>(wop + (size_t)i * DM + 8 * lane);
#pragma unroll
    for (int i = 0; i < 4; ++i) wt[i] = *reinterpret_cast<const u32x4*>(wop + (size_t)(4 * i + r) * DM + 512 + 8 * c);
    SB();
    pv<NG3>(bB, p2, acc);
  } else {
    pv<16>(bA, p0, acc);
    PIN8(acc); SB();
#pragma unroll
    for (int i = 0; i < 16; ++i) wm[i] = *reinterpret_cast<const u32x4*>(wop + (size_t)i * DM + 8 * lane);
#pragma unroll
    for (int i = 0; i < 4; ++i) wt[i] = *reinterpret_cast<const u32x4*>(wop + (size_t)(4 * i + r) * DM + 512 + 8 * c);
    SB();
    pv<16>(bB, p1, acc);
  }
#pragma unroll
  for (int k = 0; k < 8; ++k) acc[k] = xrow_sum(acc[k]);
  if (r == 0) {
    *reinterpret_cast<f32x4*>(&lds.ap[w][8 * c]) = f32x4{acc[0], acc[1], acc[2], acc[3]};
    *reinterpret_cast<f32x4*>(&lds.ap[w][8 * c + 4]) = f32x4{acc[4], acc[5], acc[6], acc[7]};
    if (c == 0) { lds.ml[w][0] = m; lds.ml[w][1] = l; }
  }
  __syncthreads();
  if (tid < DH) {
    float M = -INFINITY;
#pragma unroll
    for (int u = 0; u < NWAVE; ++u) M = fmaxf(M, lds.ml[u][0]);
    float L = 0.f, o = 0.f;
#pragma unroll
    for (int u = 0; u < NWAVE; ++u) {
      const float sc = __builtin_amdgcn_exp2f(lds.ml[u][0] - M);
      L = fmaf(sc, lds.ml[u][1], L);
      o = fmaf(sc, lds.ap[u][tid], o);
    }
    lds.o[tid] = o;
    if (tid == 0) { ml_out[0] = M; ml_out[1] = L; }
  }
  __syncthreads();
  {
    float ym[8], yt[8];
#pragma unroll
    for (int k = 0; k < 8; ++k) { ym[k] = 0.f; yt[k] = 0.f; }
    float ov[16];
#pragma unroll
    for (int i = 0; i < 4; ++i) {
      const f32x4 t = *reinterpret_cast<const f32x4*>(&lds.o[16 * w + 4 * i]);
      ov[4 * i] = t[0]; ov[4 * i + 1] = t[1]; ov[4 * i + 2] = t[2]; ov[4 * i + 3] = t[3];
    }
#pragma unroll
    for (int i = 0; i < 16; ++i)
#pragma unroll
      for (int k = 0; k < 4; ++k) {
        ym[2 * k] = fmaf(ov[i], bf_lo(wm[i][k]), ym[2 * k]);
        ym[2 * k + 1] = fmaf(ov[i], bf_hi(wm[i][k]), ym[2 * k + 1]);
      }
#pragma unroll
    for (int i = 0; i < 4; ++i) {
      const float oo = lds.o[16 * w + 4 * i + r];
#pragma unroll
      for (int k = 0; k < 4; ++k) {
        yt[2 * k] = fmaf(oo, bf_lo(wt[i][k]), yt[2 * k]);
        yt[2 * k + 1] = fmaf(oo, bf_hi(wt[i][k]), yt[2 * k + 1]);
      }
    }
#pragma unroll
    for (int k = 0; k < 8; ++k) yt[k] = xrow_sum(yt[k]);
    *reinterpret_cast<f32x4*>(&lds.yp[w][8 * lane]) = f32x4{ym[0], ym[1], ym[2], ym[3]};
    *reinterpret_cast<f32x4*>(&lds.yp[w][8 * lane + 4]) = f32x4{ym[4], ym[5], ym[6], ym[7]};
    if (r == 0) {
      *reinterpret_cast<f32x4*>(&lds.yp[w][512 + 8 * c]) = f32x4{yt[0], yt[1], yt[2], yt[3]};
      *reinterpret_cast<f32x4*>(&lds.yp[w][512 + 8 * c + 4]) = f32x4{yt[4], yt[5], yt[6], yt[7]};
    }
  }
  __syncthreads();
  if (tid < DM / 4) {
    f32x4 s = {0.f, 0.f, 0.f, 0.f};
#pragma unroll
    for (int u = 0; u < NWAVE; ++u) s += *reinterpret_cast<const f32x4*>(&lds.yp[u][4 * tid]);
    *reinterpret_cast<f32x4*>(y_out + 4 * tid) = s;
  }
}

__global__ void __launch_bounds__(NTHR, 2)
attn_main(const __hip_bfloat16* __restrict__ ax, const __hip_bfloat16* __restrict__ ak, const __hip_bfloat16* __restrict__ av,
          const __hip_bfloat16* __restrict__ awq, const __hip_bfloat16* __restrict__ awo,
          const __hip_bfloat16* __restrict__ bx, const __hip_bfloat16* __restrict__ bk, const __hip_bfloat16* __restrict__ bv,
          const __hip_bfloat16* __restrict__ bwq, const __hip_bfloat16* __restrict__ bwo,
          float* __restrict__ ys, float* __restrict__ mls) {
  __shared__ Lds lds;
  const int b = blockIdx.x, xcd = b & 7, i = b >> 3;
  const int w = __builtin_amdgcn_readfirstlane(threadIdx.x >> 6);
  if (i < 24) {
    const int h = 4 * xcd + i / SPLIT_A, j = i % SPLIT_A;
    const int gw = j * NWAVE + w;
    const int slot0 = 42 * gw + min(gw, 32), n = gw < 32 ? 43 : 42;
    const int wg = h * SPLIT_A + j;
    body<11>(ax, ak, av, awq, awo, h, slot0, n, 0, ys + (size_t)wg * DM, mls + 2 * wg, lds);
  } else {
    const int h = 4 * xcd + (i - 24) / SPLIT_B, j = (i - 24) % SPLIT_B;
    const int gw = j * NWAVE + w;
    const int wg = NWG_A + h * SPLIT_B + j;
    body<0>(bx, bk, bv, bwq, bwo, h, 32 * gw, 32, SK - WIN, ys + (size_t)wg * DM, mls + 2 * wg, lds);
  }
}

__global__ void __launch_bounds__(256)
attn_combine(const float* __restrict__ ys, const float* __restrict__ mls, __hip_bfloat16* __restrict__ out) {
  __shared__ float sc[NWG_A];
  __shared__ __attribute__((aligned(16))) float red[32][32];
  const int br = blockIdx.x / 20, cb = blockIdx.x % 20;
  const int tid = threadIdx.x, rg = tid >> 3, c4 = tid & 7;
  const int nrow = br ? NWG_B : NWG_A, row0 = br ? NWG_A : 0, ns = br ? SPLIT_B : SPLIT_A;
  f32x4 v[6];
#pragma unroll
  for (int i = 0; i < 6; ++i) {
    const int row = min(rg + 32 * i, nrow - 1);
    v[i] = *reinterpret_cast<const f32x4*>(ys + (size_t)(row0 + row) * DM + 32 * cb + 4 * c4);
  }
  if (tid < nrow) {
    const int hb = row0 + (tid / ns) * ns;
    float M = -INFINITY;
    for (int j = 0; j < ns; ++j) M = fmaxf(M, mls[2 * (hb + j)]);
    float L = 0.f;
    for (int j = 0; j < ns; ++j) L = fmaf(__builtin_amdgcn_exp2f(mls[2 * (hb + j)] - M), mls[2 * (hb + j) + 1], L);
    sc[tid] = __builtin_amdgcn_exp2f(mls[2 * (row0 + tid)] - M) / L;
  }
  __syncthreads();
  f32x4 s = {0.f, 0.f, 0.f, 0.f};
#pragma unroll
  for (int i = 0; i < 6; ++i) {
    const int row = rg + 32 * i;
    if (row < nrow) s += sc[row] * v[i];
  }
  *reinterpret_cast<f32x4*>(&red[rg][4 * c4]) = s;
  __syncthreads();
  if (tid < 32) {
    float t = 0.f;
#pragma unroll
    for (int g = 0; g < 32; ++g) t += red[g][tid];
    out[br * DM + 32 * cb + tid] = __float2bfloat16(t);
  }
}

extern "C" void kernel_launch(void* const* d_in, const int* in_sizes, int n_in,
                              void* d_out, int out_size, void* d_ws, size_t ws_size,
                              hipStream_t stream) {
  typedef const __hip_bfloat16* P;
  float* ys = (float*)d_ws;
  float* mls = ys + (size_t)NWG * DM;
  attn_main<<<NWG, NTHR, 0, stream>>>((P)d_in[0], (P)d_in[1], (P)d_in[2], (P)d_in[3], (P)d_in[4],
                                      (P)d_in[5], (P)d_in[6], (P)d_in[7], (P)d_in[8], (P)d_in[9], ys, mls);
  attn_combine<<<40, 256, 0, stream>>>(ys, mls, (__hip_bfloat16*)d_out);
}
```

```cpp
#include <hip/hip_runtime.h>
#include <hip/hip_bf16.h>
#include <stdint.h>

typedef unsigned u32x4 __attribute__((ext_vector_type(4)));
typedef unsigned u32x2 __attribute__((ext_vector_type(2)));
typedef float f32x4 __attribute__((ext_vector_type(4)));
typedef __bf16 bf16x2 __attribute__((ext_vector_type(2)));
typedef unsigned long long u64;

constexpr int DM = 640, NH = 32, DH = 128, SK = 8192, WIN = 2048;
constexpr int NWAVE = 8, NTHR = NWAVE * 64;
constexpr int SPLIT_A = 6, SPLIT_B = 2;
constexpr int NWG_A = NH * SPLIT_A, NWG_B = NH * SPLIT_B, NWG = NWG_A + NWG_B;
constexpr float SC2 = 0.088388347648318440f * 1.4426950408889634f;
constexpr int FLAG0 = 128, GRAN0 = 8192, GSTR = 648;
constexpr unsigned SPIN_CAP = 1u << 22;

#define STAMP(i)
#define STAMP_FLUSH()
#define SB() __builtin_amdgcn_sched_barrier(0)
#define PIN(x) asm volatile("" : "+v"(x))
#define PIN8(a) do { PIN(a[0]); PIN(a[1]); PIN(a[2]); PIN(a[3]); PIN(a[4]); PIN(a[5]); PIN(a[6]); PIN(a[7]); } while (0)
#define RING 24
#define PSLEEP 4
#define P1 4

__device__ __forceinline__ float dot2(unsigned k, unsigned q, float acc) {
  return __builtin_amdgcn_fdot2_f32_bf16(__builtin_bit_cast(bf16x2, k), __builtin_bit_cast(bf16x2, q), acc, false);
}
template <int CTRL> __device__ __forceinline__ float dpp(float x) {
  return __builtin_bit_cast(float, __builtin_amdgcn_mov_dpp(__builtin_bit_cast(int, x), CTRL, 0xf, 0xf, true));
}
constexpr int XOR1 = 0xB1, XOR2 = 0x4E, XOR7 = 0x141, XOR8 = 0x128;
template <int T> __device__ __forceinline__ float bcast16(float p) { return dpp<0x150 + T>(p); }
__device__ __forceinline__ float xrow_max(float x) {
  auto s = __builtin_amdgcn_permlane16_swap(__float_as_uint(x), __float_as_uint(x), false, false);
  x = fmaxf(__uint_as_float(s[0]), __uint_as_float(s[1]));
  auto t = __builtin_amdgcn_permlane32_swap(__float_as_uint(x), __float_as_uint(x), false, false);
  return fmaxf(__uint_as_float(t[0]), __uint_as_float(t[1]));
}
__device__ __forceinline__ float xrow_sum(float x) {
  auto s = __builtin_amdgcn_permlane16_swap(__float_as_uint(x), __float_as_uint(x), false, false);
  x = __uint_as_float(s[0]) + __uint_as_float(s[1]);
  auto t = __builtin_amdgcn_permlane32_swap(__float_as_uint(x), __float_as_uint(x), false, false);
  return __uint_as_float(t[0]) + __uint_as_float(t[1]);
}
__device__ __forceinline__ float wave_max(float x) {
  x = fmaxf(x, dpp<XOR1>(x)); x = fmaxf(x, dpp<XOR2>(x)); x = fmaxf(x, dpp<XOR7>(x)); x = fmaxf(x, dpp<XOR8>(x));
  return xrow_max(x);
}
__device__ __forceinline__ float wave_sum(float x) {
  x += dpp<XOR1>(x); x += dpp<XOR2>(x); x += dpp<XOR7>(x); x += dpp<XOR8>(x);
  return xrow_sum(x);
}
__device__ __forceinline__ float bf_lo(unsigned u) { return __uint_as_float(u << 16); }
__device__ __forceinline__ float bf_hi(unsigned u) { return __uint_as_float(u & 0xffff0000u); }
__device__ __forceinline__ unsigned pack_bf16(float lo, float hi) {
  unsigned r; asm("v_cvt_pk_bf16_f32 %0, %1, %2" : "=v"(r) : "v"(lo), "v"(hi)); return r;
}
__device__ __forceinline__ u32x4 ldnt(const u32x4* p) { return __builtin_nontemporal_load(p); }
__device__ __forceinline__ u64 ld_g(const u64* p) { return __hip_atomic_load(p, __ATOMIC_RELAXED, __HIP_MEMORY_SCOPE_AGENT); }
__device__ __forceinline__ void st_g(u64* p, u64 v) { __hip_atomic_store(p, v, __ATOMIC_RELAXED, __HIP_MEMORY_SCOPE_AGENT); }
__device__ __forceinline__ u64 gran(float v) { return (1ull << 32) | (u64)__float_as_uint(v); }

__device__ __forceinline__ float transpose_reduce16(float (&x)[16], int c) {
#define TR_STEP(HALF, CTRL, BIT)                                                          \
  _Pragma("unroll") for (int h = 0; h < (HALF); ++h) {                                    \
    const float lo_ = x[h], hi_ = x[h + (HALF)];                                          \
    const float t1_ = lo_ + dpp<CTRL>(lo_), t2_ = hi_ + dpp<CTRL>(hi_);                   \
    x[h] = (c & (BIT)) ? t2_ : t1_; }
  TR_STEP(8, XOR8, 8) TR_STEP(4, XOR7, 4) TR_STEP(2, XOR2, 2) TR_STEP(1, XOR1, 1)
#undef TR_STEP
  return x[0];
}

struct __attribute__((aligned(16))) Lds {
  float qp[NWAVE][DH];
  float ap[NWAVE][DH];
  float ml[NWAVE][2];
  float yp[NWAVE][DM];
  float pm[NWG_A], pl[NWG_A];
  float red[NWAVE][8];
};

struct St {
  u32x4 ring[RING];
  float x[16], s[3], p[3], m, l, acc[8];
  u32x4 wm[16], wt[4];
  u64 fl;
};

template <int NS, int I>
__device__ __forceinline__ void stream(St& st, const u32x4* kp, const u32x4* vp, int last, int n, int lane, const u32x4 q,
                                       const __hip_bfloat16* wop, const u64* flagp) {
  if constexpr (I < 2 * NS) {
    const int c = lane & 15;
    const u32x4 d = st.ring[I % RING];
    if constexpr (I < NS) {
      float t = 0.f;
#pragma unroll
      for (int k = 0; k < 4; ++k) t = dot2(d[k], q[k], t);
      st.x[I & 15] = t;
      PIN(st.x[I & 15]);
    } else {
      constexpr int J = I - NS;
      const float pt = bcast16<J & 15>(st.p[J >> 4]);
#pragma unroll
      for (int k = 0; k < 4; ++k) {
        st.acc[2 * k] = fmaf(pt, bf_lo(d[k]), st.acc[2 * k]);
        st.acc[2 * k + 1] = fmaf(pt, bf_hi(d[k]), st.acc[2 * k + 1]);
      }
      PIN8(st.acc);
    }
    SB();
    constexpr int NX = I + RING;
    if constexpr (NX < NS) st.ring[I % RING] = ldnt(kp + (size_t)min(NX, last) * 64);
    else if constexpr (NX < 2 * NS) st.ring[I % RING] = ldnt(vp + (size_t)min(NX - NS, last) * 64);
    if constexpr (I >= NS + 1 && I < NS + 21) {
      constexpr int i = I - (NS + 1);
      if constexpr (i < 16) st.wm[i] = ldnt(reinterpret_cast<const u32x4*>(wop + (size_t)i * DM + 8 * lane));
      else st.wt[i - 16] = ldnt(reinterpret_cast<const u32x4*>(wop + (size_t)(4 * (i - 16) + (lane >> 4)) * DM + 512 + 8 * c));
    }
    if constexpr (I == NS) st.fl = ld_g(flagp);
    SB();
    if constexpr (I < NS && ((I & 15) == 15 || I == NS - 1)) {
#pragma unroll
      for (int t = (I & 15) + 1; t < 16; ++t) st.x[t] = 0.f;
      const float v = transpose_reduce16(st.x, c) * SC2;
      st.s[I >> 4] = ((I & ~15) + c < n) ? v : -INFINITY;
      PIN(st.s[I >> 4]);
    }
    if constexpr (I == NS - 1) {
      st.m = wave_max(fmaxf(fmaxf(st.s[0], st.s[1]), st.s[2]));
#pragma unroll
      for (int g = 0; g < 3; ++g) st.p[g] = __builtin_amdgcn_exp2f(st.s[g] - st.m);
      st.l = wave_sum(st.p[0] + st.p[1] + st.p[2]);
      PIN(st.p[0]); PIN(st.p[1]); PIN(st.p[2]);
    }
    stream<NS, I + 1>(st, kp, vp, last, n, lane, q, wop, flagp);
  }
}

template <int NS>
__device__ __forceinline__ u64 body(const __hip_bfloat16* __restrict__ x, const __hip_bfloat16* __restrict__ kc,
                                     const __hip_bfloat16* __restrict__ vc, const __hip_bfloat16* __restrict__ wq,
                                     const __hip_bfloat16* __restrict__ wo, int h, int slot0, int n, int row_off,
                                     int wg, u64* ws, Lds& lds) {
  const int tid = threadIdx.x, lane = tid & 63, w = __builtin_amdgcn_readfirstlane(tid >> 6);
  const int r = lane >> 4, c = lane & 15;
  const int last = n - 1;
  STAMP(0);
  u64* gr = ws + GRAN0 + (size_t)GSTR * wg;
  st_g(gr + tid, 0ull);
  if (tid < DM + 2 - NTHR) st_g(gr + NTHR + tid, 0ull);
  const u64 ep = ld_g(ws);
  asm volatile("" ::: "memory");
  SB();
  const u32x4* kp = reinterpret_cast<const u32x4*>(kc + ((size_t)h * SK + row_off + 4 * (size_t)slot0) * DH) + lane;
  const u32x4* vp = reinterpret_cast<const u32x4*>(vc + ((size_t)h * SK + row_off + 4 * (size_t)slot0) * DH) + lane;

  const int d0 = 80 * w + 20 * r;
  u32x2 xr[5];
#pragma unroll
  for (int i = 0; i < 5; ++i) xr[i] = *reinterpret_cast<const u32x2*>(x + d0 + 4 * i);
  u32x4 wr[20];
#pragma unroll
  for (int i = 0; i < 20; ++i)
    wr[i] = ldnt(reinterpret_cast<const u32x4*>(wq + (size_t)(d0 + i) * (NH * DH) + h * DH + 8 * c));
  SB();
  St st;
#pragma unroll
  for (int i = 0; i < P1; ++i) { st.ring[i] = ldnt(kp + (size_t)i * 64); SB(); }
  {
    float qa[8];
#pragma unroll
    for (int k = 0; k < 8; ++k) qa[k] = 0.f;
#pragma unroll
    for (int i = 0; i < 20; ++i) {
      const unsigned xw = xr[i >> 2][(i >> 1) & 1];
      const float xv = (i & 1) ? bf_hi(xw) : bf_lo(xw);
#pragma unroll
      for (int k = 0; k < 4; ++k) {
        qa[2 * k] = fmaf(xv, bf_lo(wr[i][k]), qa[2 * k]);
        qa[2 * k + 1] = fmaf(xv, bf_hi(wr[i][k]), qa[2 * k + 1]);
      }
    }
#pragma unroll
    for (int k = 0; k < 8; ++k) qa[k] = xrow_sum(qa[k]);
    if (r == 0) {
      *reinterpret_cast<f32x4*>(&lds.qp[w][8 * c]) = f32x4{qa[0], qa[1], qa[2], qa[3]};
      *reinterpret_cast<f32x4*>(&lds.qp[w][8 * c + 4]) = f32x4{qa[4], qa[5], qa[6], qa[7]};
    }
  }
  SB();
#pragma unroll
  for (int i = P1; i < RING; ++i) { st.ring[i] = ldnt(kp + (size_t)i * 64); SB(); }
  __syncthreads();
  const u64 tag = ep + 1ull;
  if (tid == 0) st_g(ws + FLAG0 + 8 * wg, tag);
  u32x4 q;
  {
    f32x4 s0 = {0.f, 0.f, 0.f, 0.f}, s1 = {0.f, 0.f, 0.f, 0.f};
#pragma unroll
    for (int u = 0; u < NWAVE; ++u) {
      s0 += *reinterpret_cast<const f32x4*>(&lds.qp[u][8 * c]);
      s1 += *reinterpret_cast<const f32x4*>(&lds.qp[u][8 * c + 4]);
    }
    q = u32x4{pack_bf16(s0[0], s0[1]), pack_bf16(s0[2], s0[3]), pack_bf16(s1[0], s1[1]), pack_bf16(s1[2], s1[3])};
  }
  STAMP(1);
  const __hip_bfloat16* wop = wo + (size_t)(h * DH + 16 * w) * DM;
  const u64* flagp = ws + FLAG0 + 8 * (tid & 255);
  st.s[0] = st.s[1] = st.s[2] = -INFINITY;
#pragma unroll
  for (int k = 0; k < 8; ++k) st.acc[k] = 0.f;
  SB();
  stream<NS, 0>(st, kp, vp, last, n, lane, q, wop, flagp);
  STAMP(3);
#pragma unroll
  for (int k = 0; k < 8; ++k) st.acc[k] = xrow_sum(st.acc[k]);
  if (r == 0) {
    *reinterpret_cast<f32x4*>(&lds.ap[w][8 * c]) = f32x4{st.acc[0], st.acc[1], st.acc[2], st.acc[3]};
    *reinterpret_cast<f32x4*>(&lds.ap[w][8 * c + 4]) = f32x4{st.acc[4], st.acc[5], st.acc[6], st.acc[7]};
    if (c == 0) { lds.ml[w][0] = st.m; lds.ml[w][1] = st.l; }
  }
  for (unsigned sp = 0; st.fl != tag && sp < SPIN_CAP; ++sp) { __builtin_amdgcn_s_sleep(2); st.fl = ld_g(flagp); }
  STAMP(2);
  __syncthreads();
  STAMP(6);
  float Mw, Lw;
  {
    float M = -INFINITY;
#pragma unroll
    for (int u = 0; u < NWAVE; ++u) M = fmaxf(M, lds.ml[u][0]);
    float L = 0.f, o = 0.f;
#pragma unroll
    for (int u = 0; u < NWAVE; ++u) {
      const float sc = __builtin_amdgcn_exp2f(lds.ml[u][0] - M);
      L = fmaf(sc, lds.ml[u][1], L);
      o = fmaf(sc, lds.ap[u][16 * w + c], o);
    }
    Mw = M; Lw = L;
    float ym[8], yt[8], ov[16];
#pragma unroll
    for (int k = 0; k < 8; ++k) { ym[k] = 0.f; yt[k] = 0.f; }
    ov[0] = bcast16<0>(o); ov[1] = bcast16<1>(o); ov[2] = bcast16<2>(o); ov[3] = bcast16<3>(o);
    ov[4] = bcast16<4>(o); ov[5] = bcast16<5>(o); ov[6] = bcast16<6>(o); ov[7] = bcast16<7>(o);
    ov[8] = bcast16<8>(o); ov[9] = bcast16<9>(o); ov[10] = bcast16<10>(o); ov[11] = bcast16<11>(o);
    ov[12] = bcast16<12>(o); ov[13] = bcast16<13>(o); ov[14] = bcast16<14>(o); ov[15] = bcast16<15>(o);
#pragma unroll
    for (int i = 0; i < 16; ++i)
#pragma unroll
      for (int k = 0; k < 4; ++k) {
        ym[2 * k] = fmaf(ov[i], bf_lo(st.wm[i][k]), ym[2 * k]);
        ym[2 * k + 1] = fmaf(ov[i], bf_hi(st.wm[i][k]), ym[2 * k + 1]);
      }
#pragma unroll
    for (int i = 0; i < 4; ++i) {
      const float oo = (r & 2) ? ((r & 1) ? ov[4 * i + 3] : ov[4 * i + 2]) : ((r & 1) ? ov[4 * i + 1] : ov[4 * i]);
#pragma unroll
      for (int k = 0; k < 4; ++k) {
        yt[2 * k] = fmaf(oo, bf_lo(st.wt[i][k]), yt[2 * k]);
        yt[2 * k + 1] = fmaf(oo, bf_hi(st.wt[i][k]), yt[2 * k + 1]);
      }
    }
#pragma unroll
    for (int k = 0; k < 8; ++k) yt[k] = xrow_sum(yt[k]);
    *reinterpret_cast<f32x4*>(&lds.yp[w][8 * lane]) = f32x4{ym[0], ym[1], ym[2], ym[3]};
    *reinterpret_cast<f32x4*>(&lds.yp[w][8 * lane + 4]) = f32x4{ym[4], ym[5], ym[6], ym[7]};
    if (r == 0) {
      *reinterpret_cast<f32x4*>(&lds.yp[w][512 + 8 * c]) = f32x4{yt[0], yt[1], yt[2], yt[3]};
      *reinterpret_cast<f32x4*>(&lds.yp[w][512 + 8 * c + 4]) = f32x4{yt[4], yt[5], yt[6], yt[7]};
    }
  }
  __syncthreads();
  STAMP(7);
  {
    float s = 0.f;
#pragma unroll
    for (int u = 0; u < NWAVE; ++u) s += lds.yp[u][tid];
    st_g(gr + tid, gran(s));
    if (tid < DM - NTHR) {
      float s2 = 0.f;
#pragma unroll
      for (int u = 0; u < NWAVE; ++u) s2 += lds.yp[u][NTHR + tid];
      st_g(gr + NTHR + tid, gran(s2));
    }
    if (tid == NTHR - 1) { st_g(gr + DM, gran(Mw)); st_g(gr + DM + 1, gran(Lw)); }
  }
  STAMP(4);
  return tag;
}

template <int NP, int NC, int NSP>
__device__ __forceinline__ void gather(const u64* ws, int p0, int col0, __hip_bfloat16* out, Lds& lds) {
  const int tid = threadIdx.x, lane = tid & 63, w = tid >> 6;
  constexpr int NG = NP * NC, LC = NC == 4 ? 2 : 3;
  const int i0 = tid, i1 = tid + NTHR;
  const bool a0 = i0 < NG, a1 = i1 < NG, am = tid < NP;
  const u64* g0 = ws + GRAN0 + (size_t)GSTR * (p0 + (a0 ? i0 >> LC : 0)) + col0 + (i0 & (NC - 1));
  const u64* g1 = ws + GRAN0 + (size_t)GSTR * (p0 + (a1 ? i1 >> LC : 0)) + col0 + (i1 & (NC - 1));
  const u64* gm = ws + GRAN0 + (size_t)GSTR * (p0 + (am ? tid : 0)) + DM;
  u64 v0 = a0 ? 0ull : 1ull << 32, v1 = (NG > NTHR && a1) ? 0ull : 1ull << 32, vm = am ? 0ull : 1ull << 32, vl = vm;
  for (unsigned sp = 0; sp < SPIN_CAP; ++sp) {
    if ((v0 >> 32) == 0) v0 = ld_g(g0);
    if (NG > NTHR && (v1 >> 32) == 0) v1 = ld_g(g1);
    if ((vm >> 32) == 0) vm = ld_g(gm);
    if ((vl >> 32) == 0) vl = ld_g(gm + 1);
    const bool ok = ((v0 >> 32) & (v1 >> 32) & (vm >> 32) & (vl >> 32)) == 1ull;
    if (__all(ok)) break;
    __builtin_amdgcn_s_sleep(PSLEEP);
  }
  if (am) { lds.pm[tid] = __uint_as_float((unsigned)vm); lds.pl[tid] = __uint_as_float((unsigned)vl); }
  __syncthreads();
  auto scale = [&](int p) {
    const int hb = (p / NSP) * NSP;
    float M = -INFINITY;
#pragma unroll
    for (int j = 0; j < NSP; ++j) M = fmaxf(M, lds.pm[hb + j]);
    float L = 0.f;
#pragma unroll
    for (int j = 0; j < NSP; ++j) L = fmaf(__builtin_amdgcn_exp2f(lds.pm[hb + j] - M), lds.pl[hb + j], L);
    return __builtin_amdgcn_exp2f(lds.pm[p] - M) / L;
  };
  float s = 0.f;
  if (a0) s = scale(i0 >> LC) * __uint_as_float((unsigned)v0);
  if (NG > NTHR && a1) s = fmaf(scale(i1 >> LC), __uint_as_float((unsigned)v1), s);
  if (NC == 4) s += dpp<0x124>(s);
  s += dpp<XOR8>(s);
  s = xrow_sum(s);
  if (lane < NC) lds.red[w][lane] = s;
  __syncthreads();
  if (tid < NC) {
    float t = 0.f;
#pragma unroll
    for (int u = 0; u < NWAVE; ++u) t += lds.red[u][tid];
    out[col0 + tid] = __float2bfloat16(t);
  }
}

__global__ void __launch_bounds__(NTHR, 2)
attn_main(const __hip_bfloat16* __restrict__ ax, const __hip_bfloat16* __restrict__ ak, const __hip_bfloat16* __restrict__ av,
          const __hip_bfloat16* __restrict__ awq, const __hip_bfloat16* __restrict__ awo,
          const __hip_bfloat16* __restrict__ bx, const __hip_bfloat16* __restrict__ bk, const __hip_bfloat16* __restrict__ bv,
          const __hip_bfloat16* __restrict__ bwq, const __hip_bfloat16* __restrict__ bwo,
          u64* ws, __hip_bfloat16* out) {
  __shared__ Lds lds;
  const int b = blockIdx.x, xcd = b & 7, i = b >> 3;
  const int w = __builtin_amdgcn_readfirstlane(threadIdx.x >> 6);
  u64 tag;
  if (i < 24) {
    const int h = 4 * xcd + i / SPLIT_A, j = i % SPLIT_A;
    const int gw = j * NWAVE + w;
    const int slot0 = 42 * gw + min(gw, 32), n = gw < 32 ? 43 : 42;
    tag = body<43>(ax, ak, av, awq, awo, h, slot0, n, 0, h * SPLIT_A + j, ws, lds);
  } else {
    const int h = 4 * xcd + (i - 24) / SPLIT_B, j = (i - 24) % SPLIT_B;
    const int gw = j * NWAVE + w;
    tag = body<32>(bx, bk, bv, bwq, bwo, h, 32 * gw, 32, SK - WIN, NWG_A + h * SPLIT_B + j, ws, lds);
  }
  if (b < 160) gather<NWG_A, 4, SPLIT_A>(ws, 0, 4 * b, out, lds);
  else if (b < 240) gather<NWG_B, 8, SPLIT_B>(ws, NWG_A, 8 * (b - 160), out + DM, lds);
  if (b == 0 && threadIdx.x == 0) st_g(ws, tag);
  STAMP(5);
  STAMP_FLUSH();
}

extern "C" void kernel_launch(void* const* d_in, const int* in_sizes, int n_in,
                              void* d_out, int out_size, void* d_ws, size_t ws_size,
                              hipStream_t stream) {
  typedef const __hip_bfloat16* P;
  attn_main<<<NWG, NTHR, 0, stream>>>((P)d_in[0], (P)d_in[1], (P)d_in[2], (P)d_in[3], (P)d_in[4],
                                      (P)d_in[5], (P)d_in[6], (P)d_in[7], (P)d_in[8], (P)d_in[9],
                                      (u64*)d_ws, (__hip_bfloat16*)d_out);
}
```

```cpp
#include <hip/hip_runtime.h>
#include <hip/hip_bf16.h>
#include <stdint.h>

typedef unsigned u32x4 __attribute__((ext_vector_type(4)));
typedef unsigned u32x2 __attribute__((ext_vector_type(2)));
typedef float f32x4 __attribute__((ext_vector_type(4)));
typedef __bf16 bf16x2 __attribute__((ext_vector_type(2)));

constexpr int DM = 640, NH = 32, DH = 128, SK = 8192, WIN = 2048;
constexpr int NWAVE = 8, NTHR = NWAVE * 64;
constexpr int SPLIT_A = 6, SPLIT_B = 2;
constexpr int NWG_A = NH * SPLIT_A, NWG_B = NH * SPLIT_B, NWG = NWG_A + NWG_B;
constexpr float SC2 = 0.088388347648318440f * 1.4426950408889634f;

#define SB() do { asm volatile("" ::: "memory"); __builtin_amdgcn_sched_barrier(0); } while (0)
#define PIN(x) asm volatile("" : "+v"(x))
#define PIN8(a) do { PIN(a[0]); PIN(a[1]); PIN(a[2]); PIN(a[3]); PIN(a[4]); PIN(a[5]); PIN(a[6]); PIN(a[7]); } while (0)
#define P1 4

__device__ __forceinline__ float dot2(unsigned k, unsigned q, float acc) {
  return __builtin_amdgcn_fdot2_f32_bf16(__builtin_bit_cast(bf16x2, k), __builtin_bit_cast(bf16x2, q), acc, false);
}
template <int CTRL> __device__ __forceinline__ float dpp(float x) {
  return __builtin_bit_cast(float, __builtin_amdgcn_mov_dpp(__builtin_bit_cast(int, x), CTRL, 0xf, 0xf, true));
}
constexpr int XOR1 = 0xB1, XOR2 = 0x4E, XOR7 = 0x141, XOR8 = 0x128;
template <int T> __device__ __forceinline__ float bcast16(float p) { return dpp<0x150 + T>(p); }
__device__ __forceinline__ float xrow_max(float x) {
  auto s = __builtin_amdgcn_permlane16_swap(__float_as_uint(x), __float_as_uint(x), false, false);
  x = fmaxf(__uint_as_float(s[0]), __uint_as_float(s[1]));
  auto t = __builtin_amdgcn_permlane32_swap(__float_as_uint(x), __float_as_uint(x), false, false);
  return fmaxf(__uint_as_float(t[0]), __uint_as_float(t[1]));
}
__device__ __forceinline__ float xrow_sum(float x) {
  auto s = __builtin_amdgcn_permlane16_swap(__float_as_uint(x), __float_as_uint(x), false, false);
  x = __uint_as_float(s[0]) + __uint_as_float(s[1]);
  auto t = __builtin_amdgcn_permlane32_swap(__float_as_uint(x), __float_as_uint(x), false, false);
  return __uint_as_float(t[0]) + __uint_as_float(t[1]);
}
__device__ __forceinline__ float wave_max(float x) {
  x = fmaxf(x, dpp<XOR1>(x)); x = fmaxf(x, dpp<XOR2>(x)); x = fmaxf(x, dpp<XOR7>(x)); x = fmaxf(x, dpp<XOR8>(x));
  return xrow_max(x);
}
__device__ __forceinline__ float wave_sum(float x) {
  x += dpp<XOR1>(x); x += dpp<XOR2>(x); x += dpp<XOR7>(x); x += dpp<XOR8>(x);
  return xrow_sum(x);
}
__device__ __forceinline__ float bf_lo(unsigned u) { return __uint_as_float(u << 16); }
__device__ __forceinline__ float bf_hi(unsigned u) { return __uint_as_float(u & 0xffff0000u); }
__device__ __forceinline__ unsigned pack_bf16(float lo, float hi) {
  unsigned r; asm("v_cvt_pk_bf16_f32 %0, %1, %2" : "=v"(r) : "v"(lo), "v"(hi)); return r;
}
__device__ __forceinline__ u32x4 ldnt(const u32x4* p) { return __builtin_nontemporal_load(p); }

__device__ __forceinline__ float transpose_reduce16(float (&x)[16], int c) {
#define TR_STEP(HALF, CTRL, BIT)                                                          \
  _Pragma("unroll") for (int h = 0; h < (HALF); ++h) {                                    \
    const float lo_ = x[h], hi_ = x[h + (HALF)];                                          \
    const float t1_ = lo_ + dpp<CTRL>(lo_), t2_ = hi_ + dpp<CTRL>(hi_);                   \
    x[h] = (c & (BIT)) ? t2_ : t1_; }
  TR_STEP(8, XOR8, 8) TR_STEP(4, XOR7, 4) TR_STEP(2, XOR2, 2) TR_STEP(1, XOR1, 1)
#undef TR_STEP
  return x[0];
}

template <int T>
__device__ __forceinline__ void pv_step(const u32x4 d, float p, float (&acc)[8]) {
  const float pt = bcast16<T>(p);
#pragma unroll
  for (int k = 0; k < 4; ++k) {
    acc[2 * k] = fmaf(pt, bf_lo(d[k]), acc[2 * k]);
    acc[2 * k + 1] = fmaf(pt, bf_hi(d[k]), acc[2 * k + 1]);
  }
}
template <int T, typename F>
__device__ __forceinline__ void pv_group(u32x4 (&ring)[16], float p, float (&acc)[8], F&& refill) {
  if constexpr (T < 16) {
    pv_step<T>(ring[T], p, acc);
    PIN8(acc); SB();
    ring[T] = refill(T);
    SB();
    pv_group<T + 1>(ring, p, acc, refill);
  }
}

struct __attribute__((aligned(16))) Lds {
  float qp[NWAVE][DH];
  float ap[NWAVE][DH];
  float ml[NWAVE][2];
  float yp[NWAVE][DM];
};

__global__ void __launch_bounds__(NTHR, 2)
attn_main(const __hip_bfloat16* __restrict__ ax, const __hip_bfloat16* __restrict__ ak, const __hip_bfloat16* __restrict__ av,
          const __hip_bfloat16* __restrict__ awq, const __hip_bfloat16* __restrict__ awo,
          const __hip_bfloat16* __restrict__ bx, const __hip_bfloat16* __restrict__ bk, const __hip_bfloat16* __restrict__ bv,
          const __hip_bfloat16* __restrict__ bwq, const __hip_bfloat16* __restrict__ bwo,
          float* __restrict__ ys, float* __restrict__ mls) {
  __shared__ Lds lds;
  const int tid = threadIdx.x, lane = tid & 63, w = __builtin_amdgcn_readfirstlane(tid >> 6);
  const int r = lane >> 4, c = lane & 15;
  const int b = blockIdx.x, xcd = b & 7, bi = b >> 3;
  const bool isA = bi < 24;
  int h, wg, slot0, n;
  if (isA) {
    h = 4 * xcd + bi / SPLIT_A; const int j = bi % SPLIT_A, gw = j * NWAVE + w;
    slot0 = 42 * gw + min(gw, 32); n = gw < 32 ? 43 : 42; wg = h * SPLIT_A + j;
  } else {
    h = 4 * xcd + (bi - 24) / SPLIT_B; const int j = (bi - 24) % SPLIT_B, gw = j * NWAVE + w;
    slot0 = (SK - WIN) / 4 + 32 * gw; n = 32; wg = NWG_A + h * SPLIT_B + j;
  }
  const __hip_bfloat16* x = isA ? ax : bx;
  const __hip_bfloat16* wq = isA ? awq : bwq;
  const __hip_bfloat16* wo = isA ? awo : bwo;
  const int last = n - 1, G = (n + 15) >> 4;
  const u32x4* kp = reinterpret_cast<const u32x4*>((isA ? ak : bk) + ((size_t)h * SK + 4 * (size_t)slot0) * DH) + lane;
  const u32x4* vp = reinterpret_cast<const u32x4*>((isA ? av : bv) + ((size_t)h * SK + 4 * (size_t)slot0) * DH) + lane;

  const int d0 = 80 * w + 20 * r;
  u32x2 xr[5];
#pragma unroll
  for (int i = 0; i < 5; ++i) xr[i] = *reinterpret_cast<const u32x2*>(x + d0 + 4 * i);
  u32x4 wr[20];
#pragma unroll
  for (int i = 0; i < 20; ++i)
    wr[i] = ldnt(reinterpret_cast<const u32x4*>(wq + (size_t)(d0 + i) * (NH * DH) + h * DH + 8 * c));
  SB();
  u32x4 ring[16];
#pragma unroll
  for (int i = 0; i < P1; ++i) { ring[i] = ldnt(kp + (size_t)i * 64); SB(); }
  {
    float qa[8];
#pragma unroll
    for (int k = 0; k < 8; ++k) qa[k] = 0.f;
#pragma unroll
    for (int i = 0; i < 20; ++i) {
      const unsigned xw = xr[i >> 2][(i >> 1) & 1];
      const float xv = (i & 1) ? bf_hi(xw) : bf_lo(xw);
#pragma unroll
      for (int k = 0; k < 4; ++k) {
        qa[2 * k] = fmaf(xv, bf_lo(wr[i][k]), qa[2 * k]);
        qa[2 * k + 1] = fmaf(xv, bf_hi(wr[i][k]), qa[2 * k + 1]);
      }
    }
#pragma unroll
    for (int k = 0; k < 8; ++k) qa[k] = xrow_sum(qa[k]);
    if (r == 0) {
      *reinterpret_cast<f32x4*>(&lds.qp[w][8 * c]) = f32x4{qa[0], qa[1], qa[2], qa[3]};
      *reinterpret_cast<f32x4*>(&lds.qp[w][8 * c + 4]) = f32x4{qa[4], qa[5], qa[6], qa[7]};
    }
  }
  SB();
#pragma unroll
  for (int i = P1; i < 16; ++i) { ring[i] = ldnt(kp + (size_t)i * 64); SB(); }
  __syncthreads();
  u32x4 q;
  {
    f32x4 s0 = {0.f, 0.f, 0.f, 0.f}, s1 = {0.f, 0.f, 0.f, 0.f};
#pragma unroll
    for (int u = 0; u < NWAVE; ++u) {
      s0 += *reinterpret_cast<const f32x4*>(&lds.qp[u][8 * c]);
      s1 += *reinterpret_cast<const f32x4*>(&lds.qp[u][8 * c + 4]);
    }
    q = u32x4{pack_bf16(s0[0], s0[1]), pack_bf16(s0[2], s0[3]), pack_bf16(s1[0], s1[1]), pack_bf16(s1[2], s1[3])};
  }
  SB();
  float s0 = -INFINITY, s1 = -INFINITY, s2 = -INFINITY;
#pragma unroll 1
  for (int g = 0; g < G; ++g) {
    const bool more = g + 1 < G;
    const u32x4* np = more ? kp : vp;
    const int nb = more ? 16 * (g + 1) : 0;
    float xx[16];
#pragma unroll
    for (int t = 0; t < 16; ++t) {
      float d = 0.f;
#pragma unroll
      for (int k = 0; k < 4; ++k) d = dot2(ring[t][k], q[k], d);
      xx[t] = d;
      PIN(xx[t]); SB();
      ring[t] = ldnt(np + (size_t)min(nb + t, last) * 64);
      SB();
    }
    float v = transpose_reduce16(xx, c) * SC2;
    v = (16 * g + c < n) ? v : -INFINITY;
    s2 = s1; s1 = s0; s0 = v;
  }
  const float m = wave_max(fmaxf(fmaxf(s0, s1), s2));
  const float p0 = __builtin_amdgcn_exp2f(s0 - m), p1 = __builtin_amdgcn_exp2f(s1 - m), p2 = __builtin_amdgcn_exp2f(s2 - m);
  const float l = wave_sum(p0 + p1 + p2);
  float acc[8];
#pragma unroll
  for (int k = 0; k < 8; ++k) acc[k] = 0.f;
#pragma unroll 1
  for (int g = 0; g + 1 < G; ++g) {
    const int nb = 16 * (g + 1);
    pv_group<0>(ring, G - 1 - g == 2 ? p2 : p1, acc, [&](int t) { return ldnt(vp + (size_t)min(nb + t, last) * 64); });
  }
  const __hip_bfloat16* wop = wo + (size_t)(h * DH + 16 * w) * DM;
  pv_group<0>(ring, p0, acc, [&](int t) { return ldnt(reinterpret_cast<const u32x4*>(wop + (size_t)t * DM + 8 * lane)); });
  u32x4 wt[4];
#pragma unroll
  for (int i = 0; i < 4; ++i) wt[i] = ldnt(reinterpret_cast<const u32x4*>(wop + (size_t)(4 * i + r) * DM + 512 + 8 * c));
  SB();
#pragma unroll
  for (int k = 0; k < 8; ++k) acc[k] = xrow_sum(acc[k]);
  if (r == 0) {
    *reinterpret_cast<f32x4*>(&lds.ap[w][8 * c]) = f32x4{acc[0], acc[1], acc[2], acc[3]};
    *reinterpret_cast<f32x4*>(&lds.ap[w][8 * c + 4]) = f32x4{acc[4], acc[5], acc[6], acc[7]};
    if (c == 0) { lds.ml[w][0] = m; lds.ml[w][1] = l; }
  }
  __syncthreads();
  float M = -INFINITY, L = 0.f;
  {
#pragma unroll
    for (int u = 0; u < NWAVE; ++u) M = fmaxf(M, lds.ml[u][0]);
    float o = 0.f;
#pragma unroll
    for (int u = 0; u < NWAVE; ++u) {
      const float sc = __builtin_amdgcn_exp2f(lds.ml[u][0] - M);
      L = fmaf(sc, lds.ml[u][1], L);
      o = fmaf(sc, lds.ap[u][16 * w + c], o);
    }
    float ym[8], yt[8], ov[16];
#pragma unroll
    for (int k = 0; k < 8; ++k) { ym[k] = 0.f; yt[k] = 0.f; }
    ov[0] = bcast16<0>(o); ov[1] = bcast16<1>(o); ov[2] = bcast16<2>(o); ov[3] = bcast16<3>(o);
    ov[4] = bcast16<4>(o); ov[5] = bcast16<5>(o); ov[6] = bcast16<6>(o); ov[7] = bcast16<7>(o);
    ov[8] = bcast16<8>(o); ov[9] = bcast16<9>(o); ov[10] = bcast16<10>(o); ov[11] = bcast16<11>(o);
    ov[12] = bcast16<12>(o); ov[13] = bcast16<13>(o); ov[14] = bcast16<14>(o); ov[15] = bcast16<15>(o);
#pragma unroll
    for (int i = 0; i < 16; ++i)
#pragma unroll
      for (int k = 0; k < 4; ++k) {
        ym[2 * k] = fmaf(ov[i], bf_lo(ring[i][k]), ym[2 * k]);
        ym[2 * k + 1] = fmaf(ov[i], bf_hi(ring[i][k]), ym[2 * k + 1]);
      }
#pragma unroll
    for (int i = 0; i < 4; ++i) {
      const float oo = (r & 2) ? ((r & 1) ? ov[4 * i + 3] : ov[4 * i + 2]) : ((r & 1) ? ov[4 * i + 1] : ov[4 * i]);
#pragma unroll
      for (int k = 0; k < 4; ++k) {
        yt[2 * k] = fmaf(oo, bf_lo(wt[i][k]), yt[2 * k]);
        yt[2 * k + 1] = fmaf(oo, bf_hi(wt[i][k]), yt[2 * k + 1]);
      }
    }
#pragma unroll
    for (int k = 0; k < 8; ++k) yt[k] = xrow_sum(yt[k]);
    *reinterpret_cast<f32x4*>(&lds.yp[w][8 * lane]) = f32x4{ym[0], ym[1], ym[2], ym[3]};
    *reinterpret_cast<f32x4*>(&lds.yp[w][8 * lane + 4]) = f32x4{ym[4], ym[5], ym[6], ym[7]};
    if (r == 0) {
      *reinterpret_cast<f32x4*>(&lds.yp[w][512 + 8 * c]) = f32x4{yt[0], yt[1], yt[2], yt[3]};
      *reinterpret_cast<f32x4*>(&lds.yp[w][512 + 8 * c + 4]) = f32x4{yt[4], yt[5], yt[6], yt[7]};
    }
  }
  __syncthreads();
  if (tid < DM / 4) {
    f32x4 s = {0.f, 0.f, 0.f, 0.f};
#pragma unroll
    for (int u = 0; u < NWAVE; ++u) s += *reinterpret_cast<const f32x4*>(&lds.yp[u][4 * tid]);
    *reinterpret_cast<f32x4*>(ys + (size_t)wg * DM + 4 * tid) = s;
  }
  if (tid == NTHR - 1) { mls[2 * wg] = M; mls[2 * wg + 1] = L; }
}

__global__ void __launch_bounds__(256)
attn_combine(const float* __restrict__ ys, const float* __restrict__ mls, __hip_bfloat16* __restrict__ out) {
  __shared__ float sc[NWG_A];
  __shared__ __attribute__((aligned(16))) float red[32][32];
  const int br = blockIdx.x / 20, cb = blockIdx.x % 20;
  const int tid = threadIdx.x, rg = tid >> 3, c4 = tid & 7;
  const int nrow = br ? NWG_B : NWG_A, row0 = br ? NWG_A : 0, ns = br ? SPLIT_B : SPLIT_A;
  f32x4 v[6];
#pragma unroll
  for (int i = 0; i < 6; ++i) {
    const int row = min(rg + 32 * i, nrow - 1);
    v[i] = *reinterpret_cast<const f32x4*>(ys + (size_t)(row0 + row) * DM + 32 * cb + 4 * c4);
  }
  if (tid < nrow) {
    const int hb = row0 + (tid / ns) * ns;
    float M = -INFINITY;
    for (int j = 0; j < ns; ++j) M = fmaxf(M, mls[2 * (hb + j)]);
    float L = 0.f;
    for (int j = 0; j < ns; ++j) L = fmaf(__builtin_amdgcn_exp2f(mls[2 * (hb + j)] - M), mls[2 * (hb + j) + 1], L);
    sc[tid] = __builtin_amdgcn_exp2f(mls[2 * (row0 + tid)] - M) / L;
  }
  __syncthreads();
  f32x4 s = {0.f, 0.f, 0.f, 0.f};
#pragma unroll
  for (int i = 0; i < 6; ++i) {
    const int row = rg + 32 * i;
    if (row < nrow) s += sc[row] * v[i];
  }
  *reinterpret_cast<f32x4*>(&red[rg][4 * c4]) = s;
  __syncthreads();
  if (tid < 32) {
    float t = 0.f;
#pragma unroll
    for (int g = 0; g < 32; ++g) t += red[g][tid];
    out[br * DM + 32 * cb + tid] = __float2bfloat16(t);
  }
}

extern "C" void kernel_launch(void* const* d_in, const int* in_sizes, int n_in,
                              void* d_out, int out_size, void* d_ws, size_t ws_size,
                              hipStream_t stream) {
  typedef const __hip_bfloat16* P;
  float* ys = (float*)d_ws;
  float* mls = ys + (size_t)NWG * DM;
  attn_main<<<NWG, NTHR, 0, stream>>>((P)d_in[0], (P)d_in[1], (P)d_in[2], (P)d_in[3], (P)d_in[4],
                                      (P)d_in[5], (P)d_in[6], (P)d_in[7], (P)d_in[8], (P)d_in[9], ys, mls);
  attn_combine<<<40, 256, 0, stream>>>(ys, mls, (__hip_bfloat16*)d_out);
}
```

```cpp
#include <hip/hip_runtime.h>
#include <hip/hip_bf16.h>
#include <stdint.h>

typedef unsigned u32x4 __attribute__((ext_vector_type(4)));
typedef unsigned u32x2 __attribute__((ext_vector_type(2)));
typedef float f32x4 __attribute__((ext_vector_type(4)));
typedef __bf16 bf16x2 __attribute__((ext_vector_type(2)));

constexpr int DM = 640, NH = 32, DH = 128, SK = 8192, WIN = 2048;
constexpr int NWAVE = 8, NTHR = NWAVE * 64;
constexpr int SPLIT_A = 6, SPLIT_B = 2;
constexpr int NWG_A = NH * SPLIT_A, NWG_B = NH * SPLIT_B, NWG = NWG_A + NWG_B;
constexpr float SC2 = 0.088388347648318440f * 1.4426950408889634f;

#define SB() do { asm volatile("" ::: "memory"); __builtin_amdgcn_sched_barrier(0); } while (0)
#define PIN(x) asm volatile("" : "+v"(x))
#define PIN8(a) do { PIN(a[0]); PIN(a[1]); PIN(a[2]); PIN(a[3]); PIN(a[4]); PIN(a[5]); PIN(a[6]); PIN(a[7]); } while (0)
#define WNT 0
#define KVNT 1
#define P1 16

__device__ __forceinline__ float dot2(unsigned k, unsigned q, float acc) {
  return __builtin_amdgcn_fdot2_f32_bf16(__builtin_bit_cast(bf16x2, k), __builtin_bit_cast(bf16x2, q), acc, false);
}
template <int CTRL> __device__ __forceinline__ float dpp(float x) {
  return __builtin_bit_cast(float, __builtin_amdgcn_mov_dpp(__builtin_bit_cast(int, x), CTRL, 0xf, 0xf, true));
}
constexpr int XOR1 = 0xB1, XOR2 = 0x4E, XOR7 = 0x141, XOR8 = 0x128;
template <int T> __device__ __forceinline__ float bcast16(float p) { return dpp<0x150 + T>(p); }
__device__ __forceinline__ float xrow_max(float x) {
  auto s = __builtin_amdgcn_permlane16_swap(__float_as_uint(x), __float_as_uint(x), false, false);
  x = fmaxf(__uint_as_float(s[0]), __uint_as_float(s[1]));
  auto t = __builtin_amdgcn_permlane32_swap(__float_as_uint(x), __float_as_uint(x), false, false);
  return fmaxf(__uint_as_float(t[0]), __uint_as_float(t[1]));
}
__device__ __forceinline__ float xrow_sum(float x) {
  auto s = __builtin_amdgcn_permlane16_swap(__float_as_uint(x), __float_as_uint(x), false, false);
  x = __uint_as_float(s[0]) + __uint_as_float(s[1]);
  auto t = __builtin_amdgcn_permlane32_swap(__float_as_uint(x), __float_as_uint(x), false, false);
  return __uint_as_float(t[0]) + __uint_as_float(t[1]);
}
__device__ __forceinline__ float wave_max(float x) {
  x = fmaxf(x, dpp<XOR1>(x)); x = fmaxf(x, dpp<XOR2>(x)); x = fmaxf(x, dpp<XOR7>(x)); x = fmaxf(x, dpp<XOR8>(x));
  return xrow_max(x);
}
__device__ __forceinline__ float wave_sum(float x) {
  x += dpp<XOR1>(x); x += dpp<XOR2>(x); x += dpp<XOR7>(x); x += dpp<XOR8>(x);
  return xrow_sum(x);
}
__device__ __forceinline__ float bf_lo(unsigned u) { return __uint_as_float(u << 16); }
__device__ __forceinline__ float bf_hi(unsigned u) { return __uint_as_float(u & 0xffff0000u); }
__device__ __forceinline__ unsigned pack_bf16(float lo, float hi) {
  unsigned r; asm("v_cvt_pk_bf16_f32 %0, %1, %2" : "=v"(r) : "v"(lo), "v"(hi)); return r;
}
__device__ __forceinline__ u32x4 ldnt(const u32x4* p) { return __builtin_nontemporal_load(p); }
__device__ __forceinline__ u32x4 ldw(const u32x4* p) { return WNT ? __builtin_nontemporal_load(p) : *p; }
__device__ __forceinline__ u32x4 ldkv(const u32x4* p) { return KVNT ? __builtin_nontemporal_load(p) : *p; }

__device__ __forceinline__ float transpose_reduce16(float (&x)[16], int c) {
#define TR_STEP(HALF, CTRL, BIT)                                                          \
  _Pragma("unroll") for (int h = 0; h < (HALF); ++h) {                                    \
    const float lo_ = x[h], hi_ = x[h + (HALF)];                                          \
    const float t1_ = lo_ + dpp<CTRL>(lo_), t2_ = hi_ + dpp<CTRL>(hi_);                   \
    x[h] = (c & (BIT)) ? t2_ : t1_; }
  TR_STEP(8, XOR8, 8) TR_STEP(4, XOR7, 4) TR_STEP(2, XOR2, 2) TR_STEP(1, XOR1, 1)
#undef TR_STEP
  return x[0];
}

template <int T>
__device__ __forceinline__ void pv_step(const u32x4 d, float p, float (&acc)[8]) {
  const float pt = bcast16<T>(p);
#pragma unroll
  for (int k = 0; k < 4; ++k) {
    acc[2 * k] = fmaf(pt, bf_lo(d[k]), acc[2 * k]);
    acc[2 * k + 1] = fmaf(pt, bf_hi(d[k]), acc[2 * k + 1]);
  }
}
template <int T, typename F>
__device__ __forceinline__ void pv_group(u32x4 (&ring)[16], float p, float (&acc)[8], F&& refill) {
  if constexpr (T < 16) {
    pv_step<T>(ring[T], p, acc);
    PIN8(acc); SB();
    ring[T] = refill(T);
    SB();
    pv_group<T + 1>(ring, p, acc, refill);
  }
}

struct __attribute__((aligned(16))) Lds {
  float qp[NWAVE][DH];
  float ap[NWAVE][DH];
  float ml[NWAVE][2];
  float yp[NWAVE][DM];
};

__global__ void __launch_bounds__(NTHR, 2)
attn_main(const __hip_bfloat16* __restrict__ ax, const __hip_bfloat16* __restrict__ ak, const __hip_bfloat16* __restrict__ av,
          const __hip_bfloat16* __restrict__ awq, const __hip_bfloat16* __restrict__ awo,
          const __hip_bfloat16* __restrict__ bx, const __hip_bfloat16* __restrict__ bk, const __hip_bfloat16* __restrict__ bv,
          const __hip_bfloat16* __restrict__ bwq, const __hip_bfloat16* __restrict__ bwo,
          float* __restrict__ ys, float* __restrict__ mls) {
  __shared__ Lds lds;
  const int tid = threadIdx.x, lane = tid & 63, w = __builtin_amdgcn_readfirstlane(tid >> 6);
  const int r = lane >> 4, c = lane & 15;
  const int b = blockIdx.x, xcd = b & 7, bi = b >> 3;
  const bool isA = bi < 24;
  int h, wg, slot0, n;
  if (isA) {
    h = 4 * xcd + bi / SPLIT_A; const int j = bi % SPLIT_A, gw = j * NWAVE + w;
    slot0 = 42 * gw + min(gw, 32); n = gw < 32 ? 43 : 42; wg = h * SPLIT_A + j;
  } else {
    h = 4 * xcd + (bi - 24) / SPLIT_B; const int j = (bi - 24) % SPLIT_B, gw = j * NWAVE + w;
    slot0 = (SK - WIN) / 4 + 32 * gw; n = 32; wg = NWG_A + h * SPLIT_B + j;
  }
  const __hip_bfloat16* x = isA ? ax : bx;
  const __hip_bfloat16* wq = isA ? awq : bwq;
  const __hip_bfloat16* wo = isA ? awo : bwo;
  const int last = n - 1, G = (n + 15) >> 4;
  const u32x4* kp = reinterpret_cast<const u32x4*>((isA ? ak : bk) + ((size_t)h * SK + 4 * (size_t)slot0) * DH) + lane;
  const u32x4* vp = reinterpret_cast<const u32x4*>((isA ? av : bv) + ((size_t)h * SK + 4 * (size_t)slot0) * DH) + lane;

  const int d0 = 80 * w + 20 * r;
  u32x2 xr[5];
#pragma unroll
  for (int i = 0; i < 5; ++i) xr[i] = *reinterpret_cast<const u32x2*>(x + d0 + 4 * i);
  u32x4 wr[20];
#pragma unroll
  for (int i = 0; i < 20; ++i)
    wr[i] = ldw(reinterpret_cast<const u32x4*>(wq + (size_t)(d0 + i) * (NH * DH) + h * DH + 8 * c));
  SB();
  u32x4 ring[16];
#pragma unroll
  for (int i = 0; i < P1; ++i) { ring[i] = ldkv(kp + (size_t)i * 64); SB(); }
  {
    float qa[8];
#pragma unroll
    for (int k = 0; k < 8; ++k) qa[k] = 0.f;
#pragma unroll
    for (int i = 0; i < 20; ++i) {
      const unsigned xw = xr[i >> 2][(i >> 1) & 1];
      const float xv = (i & 1) ? bf_hi(xw) : bf_lo(xw);
#pragma unroll
      for (int k = 0; k < 4; ++k) {
        qa[2 * k] = fmaf(xv, bf_lo(wr[i][k]), qa[2 * k]);
        qa[2 * k + 1] = fmaf(xv, bf_hi(wr[i][k]), qa[2 * k + 1]);
      }
    }
#pragma unroll
    for (int k = 0; k < 8; ++k) qa[k] = xrow_sum(qa[k]);
    if (r == 0) {
      *reinterpret_cast<f32x4*>(&lds.qp[w][8 * c]) = f32x4{qa[0], qa[1], qa[2], qa[3]};
      *reinterpret_cast<f32x4*>(&lds.qp[w][8 * c + 4]) = f32x4{qa[4], qa[5], qa[6], qa[7]};
    }
  }
  SB();
#pragma unroll
  for (int i = P1; i < 16; ++i) { ring[i] = ldkv(kp + (size_t)i * 64); SB(); }
  __syncthreads();
  u32x4 q;
  {
    f32x4 s0 = {0.f, 0.f, 0.f, 0.f}, s1 = {0.f, 0.f, 0.f, 0.f};
#pragma unroll
    for (int u = 0; u < NWAVE; ++u) {
      s0 += *reinterpret_cast<const f32x4*>(&lds.qp[u][8 * c]);
      s1 += *reinterpret_cast<const f32x4*>(&lds.qp[u][8 * c + 4]);
    }
    q = u32x4{pack_bf16(s0[0], s0[1]), pack_bf16(s0[2], s0[3]), pack_bf16(s1[0], s1[1]), pack_bf16(s1[2], s1[3])};
  }
  SB();
  float s0 = -INFINITY, s1 = -INFINITY, s2 = -INFINITY;
#pragma unroll 1
  for (int g = 0; g < G; ++g) {
    const bool more = g + 1 < G;
    const u32x4* np = more ? kp : vp;
    const int nb = more ? 16 * (g + 1) : 0;
    float xx[16];
#pragma unroll
    for (int t = 0; t < 16; ++t) {
      float d = 0.f;
#pragma unroll
      for (int k = 0; k < 4; ++k) d = dot2(ring[t][k], q[k], d);
      xx[t] = d;
      PIN(xx[t]); SB();
      ring[t] = ldkv(np + (size_t)min(nb + t, last) * 64);
      SB();
    }
    float v = transpose_reduce16(xx, c) * SC2;
    v = (16 * g + c < n) ? v : -INFINITY;
    s2 = s1; s1 = s0; s0 = v;
  }
  const float m = wave_max(fmaxf(fmaxf(s0, s1), s2));
  const float p0 = __builtin_amdgcn_exp2f(s0 - m), p1 = __builtin_amdgcn_exp2f(s1 - m), p2 = __builtin_amdgcn_exp2f(s2 - m);
  const float l = wave_sum(p0 + p1 + p2);
  float acc[8];
#pragma unroll
  for (int k = 0; k < 8; ++k) acc[k] = 0.f;
#pragma unroll 1
  for (int g = 0; g + 1 < G; ++g) {
    const int nb = 16 * (g + 1);
    pv_group<0>(ring, G - 1 - g == 2 ? p2 : p1, acc, [&](int t) { return ldkv(vp + (size_t)min(nb + t, last) * 64); });
  }
  const __hip_bfloat16* wop = wo + (size_t)(h * DH + 16 * w) * DM;
  pv_group<0>(ring, p0, acc, [&](int t) { return ldw(reinterpret_cast<const u32x4*>(wop + (size_t)t * DM + 8 * lane)); });
  u32x4 wt[4];
#pragma unroll
  for (int i = 0; i < 4; ++i) wt[i] = ldw(reinterpret_cast<const u32x4*>(wop + (size_t)(4 * i + r) * DM + 512 + 8 * c));
  SB();
#pragma unroll
  for (int k = 0; k < 8; ++k) acc[k] = xrow_sum(acc[k]);
  if (r == 0) {
    *reinterpret_cast<f32x4*>(&lds.ap[w][8 * c]) = f32x4{acc[0], acc[1], acc[2], acc[3]};
    *reinterpret_cast<f32x4*>(&lds.ap[w][8 * c + 4]) = f32x4{acc[4], acc[5], acc[6], acc[7]};
    if (c == 0) { lds.ml[w][0] = m; lds.ml[w][1] = l; }
  }
  __syncthreads();
  float M = -INFINITY, L = 0.f;
  {
#pragma unroll
    for (int u = 0; u < NWAVE; ++u) M = fmaxf(M, lds.ml[u][0]);
    float o = 0.f;
#pragma unroll
    for (int u = 0; u < NWAVE; ++u) {
      const float sc = __builtin_amdgcn_exp2f(lds.ml[u][0] - M);
      L = fmaf(sc, lds.ml[u][1], L);
      o = fmaf(sc, lds.ap[u][16 * w + c], o);
    }
    float ym[8], yt[8], ov[16];
#pragma unroll
    for (int k = 0; k < 8; ++k) { ym[k] = 0.f; yt[k] = 0.f; }
    ov[0] = bcast16<0>(o); ov[1] = bcast16<1>(o); ov[2] = bcast16<2>(o); ov[3] = bcast16<3>(o);
    ov[4] = bcast16<4>(o); ov[5] = bcast16<5>(o); ov[6] = bcast16<6>(o); ov[7] = bcast16<7>(o);
    ov[8] = bcast16<8>(o); ov[9] = bcast16<9>(o); ov[10] = bcast16<10>(o); ov[11] = bcast16<11>(o);
    ov[12] = bcast16<12>(o); ov[13] = bcast16<13>(o); ov[14] = bcast16<14>(o); ov[15] = bcast16<15>(o);
#pragma unroll
    for (int i = 0; i < 16; ++i)
#pragma unroll
      for (int k = 0; k < 4; ++k) {
        ym[2 * k] = fmaf(ov[i], bf_lo(ring[i][k]), ym[2 * k]);
        ym[2 * k + 1] = fmaf(ov[i], bf_hi(ring[i][k]), ym[2 * k + 1]);
      }
#pragma unroll
    for (int i = 0; i < 4; ++i) {
      const float oo = (r & 2) ? ((r & 1) ? ov[4 * i + 3] : ov[4 * i + 2]) : ((r & 1) ? ov[4 * i + 1] : ov[4 * i]);
#pragma unroll
      for (int k = 0; k < 4; ++k) {
        yt[2 * k] = fmaf(oo, bf_lo(wt[i][k]), yt[2 * k]);
        yt[2 * k + 1] = fmaf(oo, bf_hi(wt[i][k]), yt[2 * k + 1]);
      }
    }
#pragma unroll
    for (int k = 0; k < 8; ++k) yt[k] = xrow_sum(yt[k]);
    *reinterpret_cast<f32x4*>(&lds.yp[w][8 * lane]) = f32x4{ym[0], ym[1], ym[2], ym[3]};
    *reinterpret_cast<f32x4*>(&lds.yp[w][8 * lane + 4]) = f32x4{ym[4], ym[5], ym[6], ym[7]};
    if (r == 0) {
      *reinterpret_cast<f32x4*>(&lds.yp[w][512 + 8 * c]) = f32x4{yt[0], yt[1], yt[2], yt[3]};
      *reinterpret_cast<f32x4*>(&lds.yp[w][512 + 8 * c + 4]) = f32x4{yt[4], yt[5], yt[6], yt[7]};
    }
  }
  __syncthreads();
  if (tid < DM / 4) {
    f32x4 s = {0.f, 0.f, 0.f, 0.f};
#pragma unroll
    for (int u = 0; u < NWAVE; ++u) s += *reinterpret_cast<const f32x4*>(&lds.yp[u][4 * tid]);
    *reinterpret_cast<f32x4*>(ys + (size_t)wg * DM + 4 * tid) = s;
  }
  if (tid == NTHR - 1) { mls[2 * wg] = M; mls[2 * wg + 1] = L; }
}

__global__ void __launch_bounds__(256)
attn_combine(const float* __restrict__ ys, const float* __restrict__ mls, __hip_bfloat16* __restrict__ out) {
  __shared__ float sc[NWG_A];
  __shared__ __attribute__((aligned(16))) float red[32][32];
  const int br = blockIdx.x / 20, cb = blockIdx.x % 20;
  const int tid = threadIdx.x, rg = tid >> 3, c4 = tid & 7;
  const int nrow = br ? NWG_B : NWG_A, row0 = br ? NWG_A : 0, ns = br ? SPLIT_B : SPLIT_A;
  f32x4 v[6];
#pragma unroll
  for (int i = 0; i < 6; ++i) {
    const int row = min(rg + 32 * i, nrow - 1);
    v[i] = *reinterpret_cast<const f32x4*>(ys + (size_t)(row0 + row) * DM + 32 * cb + 4 * c4);
  }
  if (tid < nrow) {
    const int hb = row0 + (tid / ns) * ns;
    float M = -INFINITY;
    for (int j = 0; j < ns; ++j) M = fmaxf(M, mls[2 * (hb + j)]);
    float L = 0.f;
    for (int j = 0; j < ns; ++j) L = fmaf(__builtin_amdgcn_exp2f(mls[2 * (hb + j)] - M), mls[2 * (hb + j) + 1], L);
    sc[tid] = __builtin_amdgcn_exp2f(mls[2 * (row0 + tid)] - M) / L;
  }
  __syncthreads();
  f32x4 s = {0.f, 0.f, 0.f, 0.f};
#pragma unroll
  for (int i = 0; i < 6; ++i) {
    const int row = rg + 32 * i;
    if (row < nrow) s += sc[row] * v[i];
  }
  *reinterpret_cast<f32x4*>(&red[rg][4 * c4]) = s;
  __syncthreads();
  if (tid < 32) {
    float t = 0.f;
#pragma unroll
    for (int g = 0; g < 32; ++g) t += red[g][tid];
    out[br * DM + 32 * cb + tid] = __float2bfloat16(t);
  }
}

extern "C" void kernel_launch(void* const* d_in, const int* in_sizes, int n_in,
                              void* d_out, int out_size, void* d_ws, size_t ws_size,
                              hipStream_t stream) {
  typedef const __hip_bfloat16* P;
  float* ys = (float*)d_ws;
  float* mls = ys + (size_t)NWG * DM;
  attn_main<<<NWG, NTHR, 0, stream>>>((P)d_in[0], (P)d_in[1], (P)d_in[2], (P)d_in[3], (P)d_in[4],
                                      (P)d_in[5], (P)d_in[6], (P)d_in[7], (P)d_in[8], (P)d_in[9], ys, mls);
  attn_combine<<<40, 256, 0, stream>>>(ys, mls, (__hip_bfloat16*)d_out);
}
```

```cpp
#include <hip/hip_runtime.h>
#include <hip/hip_bf16.h>
#include <stdint.h>

typedef unsigned u32x4 __attribute__((ext_vector_type(4)));
typedef unsigned u32x2 __attribute__((ext_vector_type(2)));
typedef float f32x4 __attribute__((ext_vector_type(4)));
typedef __bf16 bf16x2 __attribute__((ext_vector_type(2)));

constexpr int DM = 640, NH = 32, DH = 128, SK = 8192, WIN = 2048;
constexpr int NWAVE = 8, NTHR = NWAVE * 64;
constexpr int SPLIT_A = 6, SPLIT_B = 2;
constexpr int NWG_A = NH * SPLIT_A, NWG_B = NH * SPLIT_B, NWG = NWG_A + NWG_B;
constexpr float SC2 = 0.088388347648318440f * 1.4426950408889634f;

#define SB() do { asm volatile("" ::: "memory"); __builtin_amdgcn_sched_barrier(0); } while (0)
#define PIN(x) asm volatile("" : "+v"(x))
#define PIN8(a) do { PIN(a[0]); PIN(a[1]); PIN(a[2]); PIN(a[3]); PIN(a[4]); PIN(a[5]); PIN(a[6]); PIN(a[7]); } while (0)
#define STAMP(i) do { SB(); unsigned long long t_ = __builtin_amdgcn_s_memrealtime(); if ((threadIdx.x & 63) == 0) stp[(blockIdx.x * 8 + (threadIdx.x >> 6)) * 8 + (i)] = t_; SB(); } while (0)
#define WNT 1
#define KVNT 1
#define P1 4

__device__ __forceinline__ float dot2(unsigned k, unsigned q, float acc) {
  return __builtin_amdgcn_fdot2_f32_bf16(__builtin_bit_cast(bf16x2, k), __builtin_bit_cast(bf16x2, q), acc, false);
}
template <int CTRL> __device__ __forceinline__ float dpp(float x) {
  return __builtin_bit_cast(float, __builtin_amdgcn_mov_dpp(__builtin_bit_cast(int, x), CTRL, 0xf, 0xf, true));
}
constexpr int XOR1 = 0xB1, XOR2 = 0x4E, XOR7 = 0x141, XOR8 = 0x128;
template <int T> __device__ __forceinline__ float bcast16(float p) { return dpp<0x150 + T>(p); }
__device__ __forceinline__ float xrow_max(float x) {
  auto s = __builtin_amdgcn_permlane16_swap(__float_as_uint(x), __float_as_uint(x), false, false);
  x = fmaxf(__uint_as_float(s[0]), __uint_as_float(s[1]));
  auto t = __builtin_amdgcn_permlane32_swap(__float_as_uint(x), __float_as_uint(x), false, false);
  return fmaxf(__uint_as_float(t[0]), __uint_as_float(t[1]));
}
__device__ __forceinline__ float xrow_sum(float x) {
  auto s = __builtin_amdgcn_permlane16_swap(__float_as_uint(x), __float_as_uint(x), false, false);
  x = __uint_as_float(s[0]) + __uint_as_float(s[1]);
  auto t = __builtin_amdgcn_permlane32_swap(__float_as_uint(x), __float_as_uint(x), false, false);
  return __uint_as_float(t[0]) + __uint_as_float(t[1]);
}
__device__ __forceinline__ float wave_max(float x) {
  x = fmaxf(x, dpp<XOR1>(x)); x = fmaxf(x, dpp<XOR2>(x)); x = fmaxf(x, dpp<XOR7>(x)); x = fmaxf(x, dpp<XOR8>(x));
  return xrow_max(x);
}
__device__ __forceinline__ float wave_sum(float x) {
  x += dpp<XOR1>(x); x += dpp<XOR2>(x); x += dpp<XOR7>(x); x += dpp<XOR8>(x);
  return xrow_sum(x);
}
__device__ __forceinline__ float bf_lo(unsigned u) { return __uint_as_float(u << 16); }
__device__ __forceinline__ float bf_hi(unsigned u) { return __uint_as_float(u & 0xffff0000u); }
__device__ __forceinline__ unsigned pack_bf16(float lo, float hi) {
  unsigned r; asm("v_cvt_pk_bf16_f32 %0, %1, %2" : "=v"(r) : "v"(lo), "v"(hi)); return r;
}
__device__ __forceinline__ u32x4 ldnt(const u32x4* p) { return __builtin_nontemporal_load(p); }
__device__ __forceinline__ u32x4 ldw(const u32x4* p) { return WNT ? __builtin_nontemporal_load(p) : *p; }
__device__ __forceinline__ u32x4 ldkv(const u32x4* p) { return KVNT ? __builtin_nontemporal_load(p) : *p; }

__device__ __forceinline__ float transpose_reduce16(float (&x)[16], int c) {
#define TR_STEP(HALF, CTRL, BIT)                                                          \
  _Pragma("unroll") for (int h = 0; h < (HALF); ++h) {                                    \
    const float lo_ = x[h], hi_ = x[h + (HALF)];                                          \
    const float t1_ = lo_ + dpp<CTRL>(lo_), t2_ = hi_ + dpp<CTRL>(hi_);                   \
    x[h] = (c & (BIT)) ? t2_ : t1_; }
  TR_STEP(8, XOR8, 8) TR_STEP(4, XOR7, 4) TR_STEP(2, XOR2, 2) TR_STEP(1, XOR1, 1)
#undef TR_STEP
  return x[0];
}

template <int T>
__device__ __forceinline__ void pv_step(const u32x4 d, float p, float (&acc)[8]) {
  const float pt = bcast16<T>(p);
#pragma unroll
  for (int k = 0; k < 4; ++k) {
    acc[2 * k] = fmaf(pt, bf_lo(d[k]), acc[2 * k]);
    acc[2 * k + 1] = fmaf(pt, bf_hi(d[k]), acc[2 * k + 1]);
  }
}
template <int T, typename F>
__device__ __forceinline__ void pv_group(u32x4 (&ring)[16], float p, float (&acc)[8], F&& refill) {
  if constexpr (T < 16) {
    pv_step<T>(ring[T], p, acc);
    PIN8(acc); SB();
    ring[T] = refill(T);
    SB();
    pv_group<T + 1>(ring, p, acc, refill);
  }
}

struct __attribute__((aligned(16))) Lds {
  float qp[NWAVE][DH];
  float ap[NWAVE][DH];
  float ml[NWAVE][2];
  float yp[NWAVE][DM];
};

__global__ void __launch_bounds__(NTHR, 2)
attn_main(const __hip_bfloat16* __restrict__ ax, const __hip_bfloat16* __restrict__ ak, const __hip_bfloat16* __restrict__ av,
          const __hip_bfloat16* __restrict__ awq, const __hip_bfloat16* __restrict__ awo,
          const __hip_bfloat16* __restrict__ bx, const __hip_bfloat16* __restrict__ bk, const __hip_bfloat16* __restrict__ bv,
          const __hip_bfloat16* __restrict__ bwq, const __hip_bfloat16* __restrict__ bwo,
          float* __restrict__ ys, float* __restrict__ mls, unsigned long long* __restrict__ stp) {
  __shared__ Lds lds;
  STAMP(0);
  const int tid = threadIdx.x, lane = tid & 63, w = __builtin_amdgcn_readfirstlane(tid >> 6);
  const int r = lane >> 4, c = lane & 15;
  const int b = blockIdx.x, xcd = b & 7, bi = b >> 3;
  const bool isA = bi < 24;
  int h, wg, slot0, n;
  if (isA) {
    h = 4 * xcd + bi / SPLIT_A; const int j = bi % SPLIT_A, gw = j * NWAVE + w;
    slot0 = 42 * gw + min(gw, 32); n = gw < 32 ? 43 : 42; wg = h * SPLIT_A + j;
  } else {
    h = 4 * xcd + (bi - 24) / SPLIT_B; const int j = (bi - 24) % SPLIT_B, gw = j * NWAVE + w;
    slot0 = (SK - WIN) / 4 + 32 * gw; n = 32; wg = NWG_A + h * SPLIT_B + j;
  }
  const __hip_bfloat16* x = isA ? ax : bx;
  const __hip_bfloat16* wq = isA ? awq : bwq;
  const __hip_bfloat16* wo = isA ? awo : bwo;
  const int last = n - 1, G = (n + 15) >> 4;
  const u32x4* kp = reinterpret_cast<const u32x4*>((isA ? ak : bk) + ((size_t)h * SK + 4 * (size_t)slot0) * DH) + lane;
  const u32x4* vp = reinterpret_cast<const u32x4*>((isA ? av : bv) + ((size_t)h * SK + 4 * (size_t)slot0) * DH) + lane;

  const int d0 = 80 * w + 20 * r;
  u32x2 xr[5];
#pragma unroll
  for (int i = 0; i < 5; ++i) xr[i] = *reinterpret_cast<const u32x2*>(x + d0 + 4 * i);
  u32x4 wr[20];
#pragma unroll
  for (int i = 0; i < 20; ++i)
    wr[i] = ldw(reinterpret_cast<const u32x4*>(wq + (size_t)(d0 + i) * (NH * DH) + h * DH + 8 * c));
  SB();
  u32x4 ring[16];
#pragma unroll
  for (int i = 0; i < P1; ++i) { ring[i] = ldkv(kp + (size_t)i * 64); SB(); }
  {
    float qa[8];
#pragma unroll
    for (int k = 0; k < 8; ++k) qa[k] = 0.f;
#pragma unroll
    for (int i = 0; i < 20; ++i) {
      const unsigned xw = xr[i >> 2][(i >> 1) & 1];
      const float xv = (i & 1) ? bf_hi(xw) : bf_lo(xw);
#pragma unroll
      for (int k = 0; k < 4; ++k) {
        qa[2 * k] = fmaf(xv, bf_lo(wr[i][k]), qa[2 * k]);
        qa[2 * k + 1] = fmaf(xv, bf_hi(wr[i][k]), qa[2 * k + 1]);
      }
    }
#pragma unroll
    for (int k = 0; k < 8; ++k) qa[k] = xrow_sum(qa[k]);
    if (r == 0) {
      *reinterpret_cast<f32x4*>(&lds.qp[w][8 * c]) = f32x4{qa[0], qa[1], qa[2], qa[3]};
      *reinterpret_cast<f32x4*>(&lds.qp[w][8 * c + 4]) = f32x4{qa[4], qa[5], qa[6], qa[7]};
    }
  }
  SB();
#pragma unroll
  for (int i = P1; i < 16; ++i) { ring[i] = ldkv(kp + (size_t)i * 64); SB(); }
  STAMP(1);
  __syncthreads();
  u32x4 q;
  {
    f32x4 s0 = {0.f, 0.f, 0.f, 0.f}, s1 = {0.f, 0.f, 0.f, 0.f};
#pragma unroll
    for (int u = 0; u < NWAVE; ++u) {
      s0 += *reinterpret_cast<const f32x4*>(&lds.qp[u][8 * c]);
      s1 += *reinterpret_cast<const f32x4*>(&lds.qp[u][8 * c + 4]);
    }
    q = u32x4{pack_bf16(s0[0], s0[1]), pack_bf16(s0[2], s0[3]), pack_bf16(s1[0], s1[1]), pack_bf16(s1[2], s1[3])};
  }
  SB();
  STAMP(2);
  float s0 = -INFINITY, s1 = -INFINITY, s2 = -INFINITY;
#pragma unroll 1
  for (int g = 0; g < G; ++g) {
    const bool more = g + 1 < G;
    const u32x4* np = more ? kp : vp;
    const int nb = more ? 16 * (g + 1) : 0;
    float xx[16];
#pragma unroll
    for (int t = 0; t < 16; ++t) {
      float d = 0.f;
#pragma unroll
      for (int k = 0; k < 4; ++k) d = dot2(ring[t][k], q[k], d);
      xx[t] = d;
      PIN(xx[t]); SB();
      ring[t] = ldkv(np + (size_t)min(nb + t, last) * 64);
      SB();
    }
    float v = transpose_reduce16(xx, c) * SC2;
    v = (16 * g + c < n) ? v : -INFINITY;
    s2 = s1; s1 = s0; s0 = v;
  }
  const float m = wave_max(fmaxf(fmaxf(s0, s1), s2));
  const float p0 = __builtin_amdgcn_exp2f(s0 - m), p1 = __builtin_amdgcn_exp2f(s1 - m), p2 = __builtin_amdgcn_exp2f(s2 - m);
  const float l = wave_sum(p0 + p1 + p2);
  STAMP(3);
  float acc[8];
#pragma unroll
  for (int k = 0; k < 8; ++k) acc[k] = 0.f;
#pragma unroll 1
  for (int g = 0; g + 1 < G; ++g) {
    const int nb = 16 * (g + 1);
    pv_group<0>(ring, G - 1 - g == 2 ? p2 : p1, acc, [&](int t) { return ldkv(vp + (size_t)min(nb + t, last) * 64); });
  }
  const __hip_bfloat16* wop = wo + (size_t)(h * DH + 16 * w) * DM;
  pv_group<0>(ring, p0, acc, [&](int t) { return ldw(reinterpret_cast<const u32x4*>(wop + (size_t)t * DM + 8 * lane)); });
  STAMP(4);
  u32x4 wt[4];
#pragma unroll
  for (int i = 0; i < 4; ++i) wt[i] = ldw(reinterpret_cast<const u32x4*>(wop + (size_t)(4 * i + r) * DM + 512 + 8 * c));
  SB();
#pragma unroll
  for (int k = 0; k < 8; ++k) acc[k] = xrow_sum(acc[k]);
  if (r == 0) {
    *reinterpret_cast<f32x4*>(&lds.ap[w][8 * c]) = f32x4{acc[0], acc[1], acc[2], acc[3]};
    *reinterpret_cast<f32x4*>(&lds.ap[w][8 * c + 4]) = f32x4{acc[4], acc[5], acc[6], acc[7]};
    if (c == 0) { lds.ml[w][0] = m; lds.ml[w][1] = l; }
  }
  __syncthreads();
  STAMP(5);
  float M = -INFINITY, L = 0.f;
  {
#pragma unroll
    for (int u = 0; u < NWAVE; ++u) M = fmaxf(M, lds.ml[u][0]);
    float o = 0.f;
#pragma unroll
    for (int u = 0; u < NWAVE; ++u) {
      const float sc = __builtin_amdgcn_exp2f(lds.ml[u][0] - M);
      L = fmaf(sc, lds.ml[u][1], L);
      o = fmaf(sc, lds.ap[u][16 * w + c], o);
    }
    float ym[8], yt[8], ov[16];
#pragma unroll
    for (int k = 0; k < 8; ++k) { ym[k] = 0.f; yt[k] = 0.f; }
    ov[0] = bcast16<0>(o); ov[1] = bcast16<1>(o); ov[2] = bcast16<2>(o); ov[3] = bcast16<3>(o);
    ov[4] = bcast16<4>(o); ov[5] = bcast16<5>(o); ov[6] = bcast16<6>(o); ov[7] = bcast16<7>(o);
    ov[8] = bcast16<8>(o); ov[9] = bcast16<9>(o); ov[10] = bcast16<10>(o); ov[11] = bcast16<11>(o);
    ov[12] = bcast16<12>(o); ov[13] = bcast16<13>(o); ov[14] = bcast16<14>(o); ov[15] = bcast16<15>(o);
#pragma unroll
    for (int i = 0; i < 16; ++i)
#pragma unroll
      for (int k = 0; k < 4; ++k) {
        ym[2 * k] = fmaf(ov[i], bf_lo(ring[i][k]), ym[2 * k]);
        ym[2 * k + 1] = fmaf(ov[i], bf_hi(ring[i][k]), ym[2 * k + 1]);
      }
#pragma unroll
    for (int i = 0; i < 4; ++i) {
      const float oo = (r & 2) ? ((r & 1) ? ov[4 * i + 3] : ov[4 * i + 2]) : ((r & 1) ? ov[4 * i + 1] : ov[4 * i]);
#pragma unroll
      for (int k = 0; k < 4; ++k) {
        yt[2 * k] = fmaf(oo, bf_lo(wt[i][k]), yt[2 * k]);
        yt[2 * k + 1] = fmaf(oo, bf_hi(wt[i][k]), yt[2 * k + 1]);
      }
    }
#pragma unroll
    for (int k = 0; k < 8; ++k) yt[k] = xrow_sum(yt[k]);
    *reinterpret_cast<f32x4*>(&lds.yp[w][8 * lane]) = f32x4{ym[0], ym[1], ym[2], ym[3]};
    *reinterpret_cast<f32x4*>(&lds.yp[w][8 * lane + 4]) = f32x4{ym[4], ym[5], ym[6], ym[7]};
    if (r == 0) {
      *reinterpret_cast<f32x4*>(&lds.yp[w][512 + 8 * c]) = f32x4{yt[0], yt[1], yt[2], yt[3]};
      *reinterpret_cast<f32x4*>(&lds.yp[w][512 + 8 * c + 4]) = f32x4{yt[4], yt[5], yt[6], yt[7]};
    }
  }
  STAMP(6);
  __syncthreads();
  if (tid < DM / 4) {
    f32x4 s = {0.f, 0.f, 0.f, 0.f};
#pragma unroll
    for (int u = 0; u < NWAVE; ++u) s += *reinterpret_cast<const f32x4*>(&lds.yp[u][4 * tid]);
    *reinterpret_cast<f32x4*>(ys + (size_t)wg * DM + 4 * tid) = s;
  }
  if (tid == NTHR - 1) { mls[2 * wg] = M; mls[2 * wg + 1] = L; }
  STAMP(7);
}

__global__ void __launch_bounds__(256)
attn_combine(const float* __restrict__ ys, const float* __restrict__ mls, __hip_bfloat16* __restrict__ out, unsigned long long* __restrict__ stp) {
  if (threadIdx.x == 0) stp[2048 * 8 + blockIdx.x * 2] = __builtin_amdgcn_s_memrealtime();
  __shared__ float sc[NWG_A];
  __shared__ __attribute__((aligned(16))) float red[32][32];
  const int br = blockIdx.x / 20, cb = blockIdx.x % 20;
  const int tid = threadIdx.x, rg = tid >> 3, c4 = tid & 7;
  const int nrow = br ? NWG_B : NWG_A, row0 = br ? NWG_A : 0, ns = br ? SPLIT_B : SPLIT_A;
  f32x4 v[6];
#pragma unroll
  for (int i = 0; i < 6; ++i) {
    const int row = min(rg + 32 * i, nrow - 1);
    v[i] = *reinterpret_cast<const f32x4*>(ys + (size_t)(row0 + row) * DM + 32 * cb + 4 * c4);
  }
  if (tid < nrow) {
    const int hb = row0 + (tid / ns) * ns;
    float M = -INFINITY;
    for (int j = 0; j < ns; ++j) M = fmaxf(M, mls[2 * (hb + j)]);
    float L = 0.f;
    for (int j = 0; j < ns; ++j) L = fmaf(__builtin_amdgcn_exp2f(mls[2 * (hb + j)] - M), mls[2 * (hb + j) + 1], L);
    sc[tid] = __builtin_amdgcn_exp2f(mls[2 * (row0 + tid)] - M) / L;
  }
  __syncthreads();
  f32x4 s = {0.f, 0.f, 0.f, 0.f};
#pragma unroll
  for (int i = 0; i < 6; ++i) {
    const int row = rg + 32 * i;
    if (row < nrow) s += sc[row] * v[i];
  }
  *reinterpret_cast<f32x4*>(&red[rg][4 * c4]) = s;
  __syncthreads();
  if (tid < 32) {
    float t = 0.f;
#pragma unroll
    for (int g = 0; g < 32; ++g) t += red[g][tid];
    out[br * DM + 32 * cb + tid] = __float2bfloat16(t);
  }
  if (threadIdx.x == 0) stp[2048 * 8 + blockIdx.x * 2 + 1] = __builtin_amdgcn_s_memrealtime();
}
__global__ void __launch_bounds__(64) prn(const unsigned long long* __restrict__ stp) {
  if (threadIdx.x != 0) return;
  unsigned long long t0 = ~0ull;
  for (int i = 0; i < 2048; ++i) t0 = min(t0, stp[i * 8]);
  float mn[8], mx[8], av[8], avb[8];
  for (int k = 0; k < 8; ++k) {
    float a = 1e9f, b = 0.f, s = 0.f, sb = 0.f;
    for (int i = 0; i < 2048; ++i) { float v = (float)(stp[i * 8 + k] - t0) * 0.01f; a = fminf(a, v); b = fmaxf(b, v); if (((i >> 3) >> 3) < 24) s += v; else sb += v; }
    mn[k] = a; mx[k] = b; av[k] = s / 1536.f; avb[k] = sb / 512.f;
  }
  float c0 = 1e9f, c1 = 0.f, c0m = 0.f;
  for (int i = 0; i < 40; ++i) { float v = (float)(stp[2048 * 8 + 2 * i] - t0) * 0.01f; c0 = fminf(c0, v); c0m = fmaxf(c0m, v); c1 = fmaxf(c1, (float)(stp[2048 * 8 + 2 * i + 1] - t0) * 0.01f); }
  float tn = (float)(__builtin_amdgcn_s_memrealtime() - t0) * 0.01f;
  printf("ST s0 %.1f/%.1f/%.1f s1 %.1f/%.1f/%.1f s2 %.1f/%.1f/%.1f s3 %.1f/%.1f/%.1f s4 %.1f/%.1f/%.1f s5 %.1f/%.1f/%.1f s6 %.1f/%.1f/%.1f s7 %.1f/%.1f/%.1f | B s3 %.1f s4 %.1f s7 %.1f | comb %.1f-%.1f end %.1f\n",
    mn[0], av[0], mx[0], mn[1], av[1], mx[1], mn[2], av[2], mx[2], mn[3], av[3], mx[3], mn[4], av[4], mx[4], mn[5], av[5], mx[5], mn[6], av[6], mx[6], mn[7], av[7], mx[7], avb[3], avb[4], avb[7], c0, c0m, c1);
}

extern "C" void kernel_launch(void* const* d_in, const int* in_sizes, int n_in,
                              void* d_out, int out_size, void* d_ws, size_t ws_size,
                              hipStream_t stream) {
  typedef const __hip_bfloat16* P;
  float* ys = (float*)d_ws;
  float* mls = ys + (size_t)NWG * DM;
  unsigned long long* stp = (unsigned long long*)((char*)d_ws + (1 << 20));
  attn_main<<<NWG, NTHR, 0, stream>>>((P)d_in[0], (P)d_in[1], (P)d_in[2], (P)d_in[3], (P)d_in[4],
                                      (P)d_in[5], (P)d_in[6], (P)d_in[7], (P)d_in[8], (P)d_in[9], ys, mls, stp);
  attn_combine<<<40, 256, 0, stream>>>(ys, mls, (__hip_bfloat16*)d_out, stp);
  prn<<<1, 64, 0, stream>>>(stp);
}
```

```cpp
#define PROBE 0
#include <hip/hip_runtime.h>
#include <hip/hip_bf16.h>
#include <stdint.h>

typedef unsigned u32x4 __attribute__((ext_vector_type(4)));
typedef unsigned u32x2 __attribute__((ext_vector_type(2)));
typedef float f32x4 __attribute__((ext_vector_type(4)));
typedef __bf16 bf16x2 __attribute__((ext_vector_type(2)));

constexpr int DM = 640, NH = 32, DH = 128, SK = 8192, WIN = 2048;
constexpr int NWAVE = 8, NTHR = NWAVE * 64;
constexpr int SPLIT_A = 6, SPLIT_B = 2;
constexpr int NWG_A = NH * SPLIT_A, NWG_B = NH * SPLIT_B, NWG = NWG_A + NWG_B;
constexpr float SC2 = 0.088388347648318440f * 1.4426950408889634f;

#define SB() do { asm volatile("" ::: "memory"); __builtin_amdgcn_sched_barrier(0); } while (0)
#define PIN(x) asm volatile("" : "+v"(x))
#define PIN8(a) do { PIN(a[0]); PIN(a[1]); PIN(a[2]); PIN(a[3]); PIN(a[4]); PIN(a[5]); PIN(a[6]); PIN(a[7]); } while (0)
#define STAMP(i) do { SB(); unsigned long long t_ = __builtin_amdgcn_s_memrealtime(); if ((threadIdx.x & 63) == 0) stp[(blockIdx.x * 8 + (threadIdx.x >> 6)) * 8 + (i)] = t_; SB(); } while (0)
#define WNT 1
#define KVNT 1
#define P1 4

__device__ __forceinline__ float dot2(unsigned k, unsigned q, float acc) {
  return __builtin_amdgcn_fdot2_f32_bf16(__builtin_bit_cast(bf16x2, k), __builtin_bit_cast(bf16x2, q), acc, false);
}
template <int CTRL> __device__ __forceinline__ float dpp(float x) {
  return __builtin_bit_cast(float, __builtin_amdgcn_mov_dpp(__builtin_bit_cast(int, x), CTRL, 0xf, 0xf, true));
}
constexpr int XOR1 = 0xB1, XOR2 = 0x4E, XOR7 = 0x141, XOR8 = 0x128;
template <int T> __device__ __forceinline__ float bcast16(float p) { return dpp<0x150 + T>(p); }
__device__ __forceinline__ float xrow_max(float x) {
  auto s = __builtin_amdgcn_permlane16_swap(__float_as_uint(x), __float_as_uint(x), false, false);
  x = fmaxf(__uint_as_float(s[0]), __uint_as_float(s[1]));
  auto t = __builtin_amdgcn_permlane32_swap(__float_as_uint(x), __float_as_uint(x), false, false);
  return fmaxf(__uint_as_float(t[0]), __uint_as_float(t[1]));
}
__device__ __forceinline__ float xrow_sum(float x) {
  auto s = __builtin_amdgcn_permlane16_swap(__float_as_uint(x), __float_as_uint(x), false, false);
  x = __uint_as_float(s[0]) + __uint_as_float(s[1]);
  auto t = __builtin_amdgcn_permlane32_swap(__float_as_uint(x), __float_as_uint(x), false, false);
  return __uint_as_float(t[0]) + __uint_as_float(t[1]);
}
__device__ __forceinline__ float wave_max(float x) {
  x = fmaxf(x, dpp<XOR1>(x)); x = fmaxf(x, dpp<XOR2>(x)); x = fmaxf(x, dpp<XOR7>(x)); x = fmaxf(x, dpp<XOR8>(x));
  return xrow_max(x);
}
__device__ __forceinline__ float wave_sum(float x) {
  x += dpp<XOR1>(x); x += dpp<XOR2>(x); x += dpp<XOR7>(x); x += dpp<XOR8>(x);
  return xrow_sum(x);
}
__device__ __forceinline__ float bf_lo(unsigned u) { return __uint_as_float(u << 16); }
__device__ __forceinline__ float bf_hi(unsigned u) { return __uint_as_float(u & 0xffff0000u); }
__device__ __forceinline__ unsigned pack_bf16(float lo, float hi) {
  unsigned r; asm("v_cvt_pk_bf16_f32 %0, %1, %2" : "=v"(r) : "v"(lo), "v"(hi)); return r;
}
__device__ __forceinline__ u32x4 ldnt(const u32x4* p) { return __builtin_nontemporal_load(p); }
__device__ __forceinline__ u32x4 ldw(const u32x4* p) { return WNT ? __builtin_nontemporal_load(p) : *p; }
__device__ __forceinline__ u32x4 ldkv(const u32x4* p) { return KVNT ? __builtin_nontemporal_load(p) : *p; }

__device__ __forceinline__ float transpose_reduce16(float (&x)[16], int c) {
#define TR_STEP(HALF, CTRL, BIT)                                                          \
  _Pragma("unroll") for (int h = 0; h < (HALF); ++h) {                                    \
    const float lo_ = x[h], hi_ = x[h + (HALF)];                                          \
    const float t1_ = lo_ + dpp<CTRL>(lo_), t2_ = hi_ + dpp<CTRL>(hi_);                   \
    x[h] = (c & (BIT)) ? t2_ : t1_; }
  TR_STEP(8, XOR8, 8) TR_STEP(4, XOR7, 4) TR_STEP(2, XOR2, 2) TR_STEP(1, XOR1, 1)
#undef TR_STEP
  return x[0];
}

template <int T>
__device__ __forceinline__ void pv_step(const u32x4 d, float p, float (&acc)[8]) {
  const float pt = bcast16<T>(p);
#pragma unroll
  for (int k = 0; k < 4; ++k) {
    acc[2 * k] = fmaf(pt, bf_lo(d[k]), acc[2 * k]);
    acc[2 * k + 1] = fmaf(pt, bf_hi(d[k]), acc[2 * k + 1]);
  }
}
template <int T, typename F>
__device__ __forceinline__ void pv_group(u32x4 (&ring)[16], float p, float (&acc)[8], F&& refill) {
  if constexpr (T < 16) {
    pv_step<T>(ring[T], p, acc);
    PIN8(acc); SB();
    ring[T] = refill(T);
    SB();
    pv_group<T + 1>(ring, p, acc, refill);
  }
}

struct __attribute__((aligned(16))) Lds {
  float qp[NWAVE][DH];
  float ap[NWAVE][DH];
  float ml[NWAVE][2];
  float yp[NWAVE][DM];
};

__global__ void __launch_bounds__(NTHR, 2)
attn_main(const __hip_bfloat16* __restrict__ ax, const __hip_bfloat16* __restrict__ ak, const __hip_bfloat16* __restrict__ av,
          const __hip_bfloat16* __restrict__ awq, const __hip_bfloat16* __restrict__ awo,
          const __hip_bfloat16* __restrict__ bx, const __hip_bfloat16* __restrict__ bk, const __hip_bfloat16* __restrict__ bv,
          const __hip_bfloat16* __restrict__ bwq, const __hip_bfloat16* __restrict__ bwo,
          float* __restrict__ ys, float* __restrict__ mls, unsigned long long* __restrict__ stp) {
  __shared__ Lds lds;
  STAMP(0);
  const int tid = threadIdx.x, lane = tid & 63, w = __builtin_amdgcn_readfirstlane(tid >> 6);
  const int r = lane >> 4, c = lane & 15;
  const int b = blockIdx.x, xcd = b & 7, bi = b >> 3;
  const bool isA = bi < 24;
  int h, wg, slot0, n;
  if (isA) {
    h = 4 * xcd + bi / SPLIT_A; const int j = bi % SPLIT_A, gw = j * NWAVE + w;
    slot0 = 42 * gw + min(gw, 32); n = gw < 32 ? 43 : 42; wg = h * SPLIT_A + j;
  } else {
    h = 4 * xcd + (bi - 24) / SPLIT_B; const int j = (bi - 24) % SPLIT_B, gw = j * NWAVE + w;
    slot0 = (SK - WIN) / 4 + 32 * gw; n = 32; wg = NWG_A + h * SPLIT_B + j;
  }
  const __hip_bfloat16* x = isA ? ax : bx;
  const __hip_bfloat16* wq = isA ? awq : bwq;
  const __hip_bfloat16* wo = isA ? awo : bwo;
  const int last = n - 1, G = (n + 15) >> 4;
  const u32x4* kp = reinterpret_cast<const u32x4*>((isA ? ak : bk) + ((size_t)h * SK + 4 * (size_t)slot0) * DH) + lane;
  const u32x4* vp = reinterpret_cast<const u32x4*>((isA ? av : bv) + ((size_t)h * SK + 4 * (size_t)slot0) * DH) + lane;

  const int d0 = 80 * w + 20 * r;
  u32x2 xr[5];
#pragma unroll
  for (int i = 0; i < 5; ++i) xr[i] = *reinterpret_cast<const u32x2*>(x + d0 + 4 * i);
  u32x4 wr[20];
#pragma unroll
  for (int i = 0; i < 20; ++i)
    wr[i] = ldw(reinterpret_cast<const u32x4*>(wq + (size_t)(d0 + i) * (NH * DH) + h * DH + 8 * c));
  SB();
  u32x4 ring[16];
#pragma unroll
  for (int i = 0; i < P1; ++i) { ring[i] = ldkv(kp + (size_t)i * 64); SB(); }
  {
    float qa[8];
#pragma unroll
    for (int k = 0; k < 8; ++k) qa[k] = 0.f;
#pragma unroll
    for (int i = 0; i < 20; ++i) {
      const unsigned xw = xr[i >> 2][(i >> 1) & 1];
      const float xv = (i & 1) ? bf_hi(xw) : bf_lo(xw);
#pragma unroll
      for (int k = 0; k < 4; ++k) {
        qa[2 * k] = fmaf(xv, bf_lo(wr[i][k]), qa[2 * k]);
        qa[2 * k + 1] = fmaf(xv, bf_hi(wr[i][k]), qa[2 * k + 1]);
      }
    }
#pragma unroll
    for (int k = 0; k < 8; ++k) qa[k] = xrow_sum(qa[k]);
    if (r == 0) {
      *reinterpret_cast<f32x4*>(&lds.qp[w][8 * c]) = f32x4{qa[0], qa[1], qa[2], qa[3]};
      *reinterpret_cast<f32x4*>(&lds.qp[w][8 * c + 4]) = f32x4{qa[4], qa[5], qa[6], qa[7]};
    }
  }
  SB();
#pragma unroll
  for (int i = P1; i < 16; ++i) { ring[i] = ldkv(kp + (size_t)i * 64); SB(); }
  STAMP(1);
  __syncthreads();
  u32x4 q;
  {
    f32x4 s0 = {0.f, 0.f, 0.f, 0.f}, s1 = {0.f, 0.f, 0.f, 0.f};
#pragma unroll
    for (int u = 0; u < NWAVE; ++u) {
      s0 += *reinterpret_cast<const f32x4*>(&lds.qp[u][8 * c]);
      s1 += *reinterpret_cast<const f32x4*>(&lds.qp[u][8 * c + 4]);
    }
    q = u32x4{pack_bf16(s0[0], s0[1]), pack_bf16(s0[2], s0[3]), pack_bf16(s1[0], s1[1]), pack_bf16(s1[2], s1[3])};
  }
  SB();
  STAMP(2);
  float s0 = -INFINITY, s1 = -INFINITY, s2 = -INFINITY;
#pragma unroll 1
  for (int g = 0; g < G; ++g) {
    const bool more = g + 1 < G;
    const u32x4* np = more ? kp : vp;
    const int nb = more ? 16 * (g + 1) : 0;
    float xx[16];
#pragma unroll
    for (int t = 0; t < 16; ++t) {
      float d = 0.f;
#pragma unroll
      for (int k = 0; k < 4; ++k) d = dot2(ring[t][k], q[k], d);
      xx[t] = d;
      PIN(xx[t]); SB();
      ring[t] = ldkv(np + (size_t)min(nb + t, last) * 64);
      SB();
    }
    float v = transpose_reduce16(xx, c) * SC2;
    v = (16 * g + c < n) ? v : -INFINITY;
    s2 = s1; s1 = s0; s0 = v;
  }
  const float m = wave_max(fmaxf(fmaxf(s0, s1), s2));
  const float p0 = __builtin_amdgcn_exp2f(s0 - m), p1 = __builtin_amdgcn_exp2f(s1 - m), p2 = __builtin_amdgcn_exp2f(s2 - m);
  const float l = wave_sum(p0 + p1 + p2);
  STAMP(3);
  float acc[8];
#pragma unroll
  for (int k = 0; k < 8; ++k) acc[k] = 0.f;
#pragma unroll 1
  for (int g = 0; g + 1 < G; ++g) {
    const int nb = 16 * (g + 1);
    pv_group<0>(ring, G - 1 - g == 2 ? p2 : p1, acc, [&](int t) { return ldkv(vp + (size_t)min(nb + t, last) * 64); });
  }
  const __hip_bfloat16* wop = wo + (size_t)(h * DH + 16 * w) * DM;
  pv_group<0>(ring, p0, acc, [&](int t) { return ldw(reinterpret_cast<const u32x4*>(wop + (size_t)t * DM + 8 * lane)); });
  STAMP(4);
  u32x4 wt[4];
#pragma unroll
  for (int i = 0; i < 4; ++i) wt[i] = ldw(reinterpret_cast<const u32x4*>(wop + (size_t)(4 * i + r) * DM + 512 + 8 * c));
  SB();
#pragma unroll
  for (int k = 0; k < 8; ++k) acc[k] = xrow_sum(acc[k]);
  if (r == 0) {
    *reinterpret_cast<f32x4*>(&lds.ap[w][8 * c]) = f32x4{acc[0], acc[1], acc[2], acc[3]};
    *reinterpret_cast<f32x4*>(&lds.ap[w][8 * c + 4]) = f32x4{acc[4], acc[5], acc[6], acc[7]};
    if (c == 0) { lds.ml[w][0] = m; lds.ml[w][1] = l; }
  }
  __syncthreads();
  STAMP(5);
  float M = -INFINITY, L = 0.f;
  {
#pragma unroll
    for (int u = 0; u < NWAVE; ++u) M = fmaxf(M, lds.ml[u][0]);
    float o = 0.f;
#pragma unroll
    for (int u = 0; u < NWAVE; ++u) {
      const float sc = __builtin_amdgcn_exp2f(lds.ml[u][0] - M);
      L = fmaf(sc, lds.ml[u][1], L);
      o = fmaf(sc, lds.ap[u][16 * w + c], o);
    }
    float ym[8], yt[8], ov[16];
#pragma unroll
    for (int k = 0; k < 8; ++k) { ym[k] = 0.f; yt[k] = 0.f; }
    ov[0] = bcast16<0>(o); ov[1] = bcast16<1>(o); ov[2] = bcast16<2>(o); ov[3] = bcast16<3>(o);
    ov[4] = bcast16<4>(o); ov[5] = bcast16<5>(o); ov[6] = bcast16<6>(o); ov[7] = bcast16<7>(o);
    ov[8] = bcast16<8>(o); ov[9] = bcast16<9>(o); ov[10] = bcast16<10>(o); ov[11] = bcast16<11>(o);
    ov[12] = bcast16<12>(o); ov[13] = bcast16<13>(o); ov[14] = bcast16<14>(o); ov[15] = bcast16<15>(o);
#pragma unroll
    for (int i = 0; i < 16; ++i)
#pragma unroll
      for (int k = 0; k < 4; ++k) {
        ym[2 * k] = fmaf(ov[i], bf_lo(ring[i][k]), ym[2 * k]);
        ym[2 * k + 1] = fmaf(ov[i], bf_hi(ring[i][k]), ym[2 * k + 1]);
      }
#pragma unroll
    for (int i = 0; i < 4; ++i) {
      const float oo = (r & 2) ? ((r & 1) ? ov[4 * i + 3] : ov[4 * i + 2]) : ((r & 1) ? ov[4 * i + 1] : ov[4 * i]);
#pragma unroll
      for (int k = 0; k < 4; ++k) {
        yt[2 * k] = fmaf(oo, bf_lo(wt[i][k]), yt[2 * k]);
        yt[2 * k + 1] = fmaf(oo, bf_hi(wt[i][k]), yt[2 * k + 1]);
      }
    }
#pragma unroll
    for (int k = 0; k < 8; ++k) yt[k] = xrow_sum(yt[k]);
    *reinterpret_cast<f32x4*>(&lds.yp[w][8 * lane]) = f32x4{ym[0], ym[1], ym[2], ym[3]};
    *reinterpret_cast<f32x4*>(&lds.yp[w][8 * lane + 4]) = f32x4{ym[4], ym[5], ym[6], ym[7]};
    if (r == 0) {
      *reinterpret_cast<f32x4*>(&lds.yp[w][512 + 8 * c]) = f32x4{yt[0], yt[1], yt[2], yt[3]};
      *reinterpret_cast<f32x4*>(&lds.yp[w][512 + 8 * c + 4]) = f32x4{yt[4], yt[5], yt[6], yt[7]};
    }
  }
  STAMP(6);
  __syncthreads();
  if (tid < DM / 4) {
    f32x4 s = {0.f, 0.f, 0.f, 0.f};
#pragma unroll
    for (int u = 0; u < NWAVE; ++u) s += *reinterpret_cast<const f32x4*>(&lds.yp[u][4 * tid]);
    *reinterpret_cast<f32x4*>(ys + (size_t)wg * DM + 4 * tid) = s;
  }
  if (tid == NTHR - 1) { mls[2 * wg] = M; mls[2 * wg + 1] = L; }
  STAMP(7);
}

__global__ void __launch_bounds__(256)
attn_combine(const float* __restrict__ ys, const float* __restrict__ mls, __hip_bfloat16* __restrict__ out, unsigned long long* __restrict__ stp) {
  if (threadIdx.x == 0) stp[2048 * 8 + blockIdx.x * 2] = __builtin_amdgcn_s_memrealtime();
  __shared__ float sc[NWG_A];
  __shared__ __attribute__((aligned(16))) float red[32][32];
  const int br = blockIdx.x / 20, cb = blockIdx.x % 20;
  const int tid = threadIdx.x, rg = tid >> 3, c4 = tid & 7;
  const int nrow = br ? NWG_B : NWG_A, row0 = br ? NWG_A : 0, ns = br ? SPLIT_B : SPLIT_A;
  f32x4 v[6];
#pragma unroll
  for (int i = 0; i < 6; ++i) {
    const int row = min(rg + 32 * i, nrow - 1);
    v[i] = *reinterpret_cast<const f32x4*>(ys + (size_t)(row0 + row) * DM + 32 * cb + 4 * c4);
  }
  if (tid < nrow) {
    const int hb = row0 + (tid / ns) * ns;
    float M = -INFINITY;
    for (int j = 0; j < ns; ++j) M = fmaxf(M, mls[2 * (hb + j)]);
    float L = 0.f;
    for (int j = 0; j < ns; ++j) L = fmaf(__builtin_amdgcn_exp2f(mls[2 * (hb + j)] - M), mls[2 * (hb + j) + 1], L);
    sc[tid] = __builtin_amdgcn_exp2f(mls[2 * (row0 + tid)] - M) / L;
  }
  __syncthreads();
  f32x4 s = {0.f, 0.f, 0.f, 0.f};
#pragma unroll
  for (int i = 0; i < 6; ++i) {
    const int row = rg + 32 * i;
    if (row < nrow) s += sc[row] * v[i];
  }
  *reinterpret_cast<f32x4*>(&red[rg][4 * c4]) = s;
  __syncthreads();
  if (tid < 32) {
    float t = 0.f;
#pragma unroll
    for (int g = 0; g < 32; ++g) t += red[g][tid];
    out[br * DM + 32 * cb + tid] = __float2bfloat16(t);
  }
  if (threadIdx.x == 0) stp[2048 * 8 + blockIdx.x * 2 + 1] = __builtin_amdgcn_s_memrealtime();
}
__global__ void __launch_bounds__(256) prn(const unsigned long long* __restrict__ stp) {
  __shared__ unsigned long long sm[256];
  __shared__ float sa[256], sb[256], sc_[256];
  const int tid = threadIdx.x;
  unsigned long long t0 = ~0ull;
  for (int i = 0; i < 8; ++i) t0 = min(t0, stp[(tid * 8 + i) * 8]);
  sm[tid] = t0; __syncthreads();
  for (int i = 0; i < 256; ++i) t0 = min(t0, sm[i]);
  const int k = PROBE / 10, mode = PROBE % 10;
  float a = 1e9f, b = 0.f, s = 0.f;
  if (k < 8) {
    for (int i = 0; i < 8; ++i) { float v = (float)(stp[(tid * 8 + i) * 8 + k] - t0); a = fminf(a, v); b = fmaxf(b, v); if ((tid >> 3) < 24) s += v; }
  } else if (tid < 40) {
    a = (float)(stp[2048 * 8 + 2 * tid] - t0); b = (float)(stp[2048 * 8 + 2 * tid + 1] - t0);
  }
  sa[tid] = a; sb[tid] = b; sc_[tid] = s; __syncthreads();
  if (tid == 0) {
    a = 1e9f; b = 0.f; s = 0.f;
    for (int i = 0; i < 256; ++i) { a = fminf(a, sa[i]); b = fmaxf(b, sb[i]); s += sc_[i]; }
    float X = mode == 1 ? a : mode == 2 ? s / 1536.f : mode == 3 ? b : 0.f;
    unsigned long long tend = t0 + 10000ull + (unsigned long long)(10.f * X);
    while (__builtin_amdgcn_s_memrealtime() < tend) __builtin_amdgcn_s_sleep(1);
  }
}
extern "C" void kernel_launch(void* const* d_in, const int* in_sizes, int n_in,
                              void* d_out, int out_size, void* d_ws, size_t ws_size,
                              hipStream_t stream) {
  typedef const __hip_bfloat16* P;
  float* ys = (float*)d_ws;
  float* mls = ys + (size_t)NWG * DM;
  unsigned long long* stp = (unsigned long long*)((char*)d_ws + (1 << 20));
  attn_main<<<NWG, NTHR, 0, stream>>>((P)d_in[0], (P)d_in[1], (P)d_in[2], (P)d_in[3], (P)d_in[4],
                                      (P)d_in[5], (P)d_in[6], (P)d_in[7], (P)d_in[8], (P)d_in[9], ys, mls, stp);
  attn_combine<<<40, 256, 0, stream>>>(ys, mls, (__hip_bfloat16*)d_out, stp);
  prn<<<1, 256, 0, stream>>>(stp);
}
```

```cpp
#define WOLATE
#include <hip/hip_runtime.h>
#include <hip/hip_bf16.h>
#include <stdint.h>
#include <type_traits>

typedef unsigned u32x4 __attribute__((ext_vector_type(4)));
typedef unsigned u32x2 __attribute__((ext_vector_type(2)));
typedef float f32x4 __attribute__((ext_vector_type(4)));
typedef __bf16 bf16x2 __attribute__((ext_vector_type(2)));
#define LAS __attribute__((address_space(3)))

constexpr int DM = 640, NH = 32, DH = 128, SK = 8192, WIN = 2048;
typedef unsigned long long u64;
constexpr int NCONS = 8, NLOAD = 2, NTHR = (NCONS + NLOAD + 1) * 64;
constexpr int SPLIT_A = 6, SPLIT_B = 2;
constexpr int NWG_A = NH * SPLIT_A, NWG_B = NH * SPLIT_B, NWG = NWG_A + NWG_B;
constexpr float SC2 = 0.088388347648318440f * 1.4426950408889634f;
#define RR 18
#define IR 5
#define WOG_A 2
#define PACEB 134
#define PS1 1
#define PS2 1
#define WNT 1
constexpr int ROWB = 8192;
constexpr unsigned SPIN_CAP = 1u << 20;
constexpr int QP_OFF = RR * ROWB, AP_OFF = QP_OFF + 4096, ML_OFF = AP_OFF + 4096, QF_OFF = ML_OFF + 64, CT_OFF = QF_OFF + 256, LDS_BYTES = CT_OFF + 64;
static_assert(LDS_BYTES <= 163840 && RR * ROWB >= NCONS * 320 * 4, "LDS");
constexpr int FLAG0 = 64, GR0 = 512, GSTR = 136, Y0 = GR0 + GSTR * NWG;

#define SB() do { asm volatile("" ::: "memory"); __builtin_amdgcn_sched_barrier(0); } while (0)
#define PIN(x) asm volatile("" : "+v"(x))
#define LGKM0() asm volatile("s_waitcnt lgkmcnt(0)" ::: "memory")
#define BAR() asm volatile("s_waitcnt lgkmcnt(0)\n\ts_barrier" ::: "memory")
#define STAMP(i)

__device__ __forceinline__ float dot2(unsigned k, unsigned q, float acc) {
  return __builtin_amdgcn_fdot2_f32_bf16(__builtin_bit_cast(bf16x2, k), __builtin_bit_cast(bf16x2, q), acc, false);
}
template <int CTRL> __device__ __forceinline__ float dpp(float x) {
  return __builtin_bit_cast(float, __builtin_amdgcn_mov_dpp(__builtin_bit_cast(int, x), CTRL, 0xf, 0xf, true));
}
constexpr int XOR1 = 0xB1, XOR2 = 0x4E, XOR7 = 0x141, XOR8 = 0x128;
template <int T> __device__ __forceinline__ float bcast16(float p) { return dpp<0x150 + T>(p); }
__device__ __forceinline__ float xrow_max(float x) {
  auto s = __builtin_amdgcn_permlane16_swap(__float_as_uint(x), __float_as_uint(x), false, false);
  x = fmaxf(__uint_as_float(s[0]), __uint_as_float(s[1]));
  auto t = __builtin_amdgcn_permlane32_swap(__float_as_uint(x), __float_as_uint(x), false, false);
  return fmaxf(__uint_as_float(t[0]), __uint_as_float(t[1]));
}
__device__ __forceinline__ float xrow_sum(float x) {
  auto s = __builtin_amdgcn_permlane16_swap(__float_as_uint(x), __float_as_uint(x), false, false);
  x = __uint_as_float(s[0]) + __uint_as_float(s[1]);
  auto t = __builtin_amdgcn_permlane32_swap(__float_as_uint(x), __float_as_uint(x), false, false);
  return __uint_as_float(t[0]) + __uint_as_float(t[1]);
}
__device__ __forceinline__ float wave_max(float x) {
  x = fmaxf(x, dpp<XOR1>(x)); x = fmaxf(x, dpp<XOR2>(x)); x = fmaxf(x, dpp<XOR7>(x)); x = fmaxf(x, dpp<XOR8>(x));
  return xrow_max(x);
}
__device__ __forceinline__ float wave_sum(float x) {
  x += dpp<XOR1>(x); x += dpp<XOR2>(x); x += dpp<XOR7>(x); x += dpp<XOR8>(x);
  return xrow_sum(x);
}
__device__ __forceinline__ float bf_lo(unsigned u) { return __uint_as_float(u << 16); }
__device__ __forceinline__ float bf_hi(unsigned u) { return __uint_as_float(u & 0xffff0000u); }
__device__ __forceinline__ unsigned pack_bf16(float lo, float hi) {
  unsigned r; asm("v_cvt_pk_bf16_f32 %0, %1, %2" : "=v"(r) : "v"(lo), "v"(hi)); return r;
}
__device__ __forceinline__ u64 ld_g(const u64* p) { return __hip_atomic_load(p, __ATOMIC_RELAXED, __HIP_MEMORY_SCOPE_AGENT); }
__device__ __forceinline__ void st_g(u64* p, u64 v) { __hip_atomic_store(p, v, __ATOMIC_RELAXED, __HIP_MEMORY_SCOPE_AGENT); }
__device__ __forceinline__ void st_g16(u32x4* p, float v0, float v1, u32x2 tag) {
  const u32x4 v = {__float_as_uint(v0), tag[0], __float_as_uint(v1), tag[1]};
  asm volatile("global_store_dwordx4 %0, %1, off sc1\n\ts_nop 1" :: "v"(p), "v"(v) : "memory");
}
__device__ __forceinline__ void ld_g16x2(const u32x4* p0, const u32x4* p1, u32x4& a, u32x4& b) {
  asm volatile("global_load_dwordx4 %0, %2, off sc1\n\tglobal_load_dwordx4 %1, %3, off sc1\n\ts_waitcnt vmcnt(0)"
               : "=&v"(a), "=&v"(b) : "v"(p0), "v"(p1) : "memory");
}
__device__ __forceinline__ void ld_g16x4(const u32x4* p0, const u32x4* p1, const u32x4* p2, const u32x4* p3, u32x4& a, u32x4& b, u32x4& c, u32x4& d) {
  asm volatile("global_load_dwordx4 %0, %4, off sc1\n\tglobal_load_dwordx4 %1, %5, off sc1\n\t"
               "global_load_dwordx4 %2, %6, off sc1\n\tglobal_load_dwordx4 %3, %7, off sc1\n\ts_waitcnt vmcnt(0)"
               : "=&v"(a), "=&v"(b), "=&v"(c), "=&v"(d) : "v"(p0), "v"(p1), "v"(p2), "v"(p3) : "memory");
}
__device__ __forceinline__ void ld_g16x6(const u32x4* p, const int (&o)[6], u32x4 (&v)[6]) {
  asm volatile("global_load_dwordx4 %0, %6, off sc1\n\tglobal_load_dwordx4 %1, %7, off sc1\n\t"
               "global_load_dwordx4 %2, %8, off sc1\n\tglobal_load_dwordx4 %3, %9, off sc1\n\t"
               "global_load_dwordx4 %4, %10, off sc1\n\tglobal_load_dwordx4 %5, %11, off sc1\n\ts_waitcnt vmcnt(0)"
               : "=&v"(v[0]), "=&v"(v[1]), "=&v"(v[2]), "=&v"(v[3]), "=&v"(v[4]), "=&v"(v[5])
               : "v"(p + o[0]), "v"(p + o[1]), "v"(p + o[2]), "v"(p + o[3]), "v"(p + o[4]), "v"(p + o[5]) : "memory");
}
__device__ __forceinline__ bool gok(const u32x4 g, u32x2 tag) { return g[1] == tag[0] && g[3] == tag[1]; }
__device__ __forceinline__ u32x4 ldw(const u32x4* p) { return WNT ? __builtin_nontemporal_load(p) : *p; }

__device__ __forceinline__ float transpose_reduce16(float (&x)[16], int c) {
#define TR_STEP(HALF, CTRL, BIT)                                                          \
  _Pragma("unroll") for (int h = 0; h < (HALF); ++h) {                                    \
    const float lo_ = x[h], hi_ = x[h + (HALF)];                                          \
    const float t1_ = lo_ + dpp<CTRL>(lo_), t2_ = hi_ + dpp<CTRL>(hi_);                   \
    x[h] = (c & (BIT)) ? t2_ : t1_; }
  TR_STEP(8, XOR8, 8) TR_STEP(4, XOR7, 4) TR_STEP(2, XOR2, 2) TR_STEP(1, XOR1, 1)
#undef TR_STEP
  return x[0];
}
template <int T>
__device__ __forceinline__ void pv_step(const u32x4 d, float p, float (&acc)[8]) {
  const float pt = bcast16<T>(p);
#pragma unroll
  for (int k = 0; k < 4; ++k) {
    acc[2 * k] = fmaf(pt, bf_lo(d[k]), acc[2 * k]);
    acc[2 * k + 1] = fmaf(pt, bf_hi(d[k]), acc[2 * k + 1]);
  }
}

__device__ __forceinline__ void dma4k(const void* gsrc, unsigned lds_dst) {
  unsigned keep;
  asm volatile("s_mov_b32 %0, m0\n\ts_mov_b32 m0, %2\n\ts_nop 0\n\t"
               "global_load_lds_dwordx4 %1, off nt\n\t"
               "global_load_lds_dwordx4 %1, off offset:1024 nt\n\t"
               "global_load_lds_dwordx4 %1, off offset:2048 nt\n\t"
               "global_load_lds_dwordx4 %1, off offset:3072 nt\n\t"
               "s_mov_b32 m0, %0" : "=&s"(keep) : "v"(gsrc), "s"(lds_dst) : "memory");
}
template <int N> __device__ __forceinline__ void vmw() { asm volatile("s_waitcnt vmcnt(%0)" :: "n"(N) : "memory"); }
template <int K> __device__ __forceinline__ void drain_ladder(volatile LAS unsigned* full, int total, int pub) {
  if constexpr (K >= 0) { vmw<4 * K>(); if (total - K > pub) *full = total - K; drain_ladder<K - 1>(full, total, pub); }
}
__device__ __forceinline__ int wrapr(int s) { return s >= RR ? s - RR : s; }

__global__ void __launch_bounds__(NTHR)
attn_main(const __hip_bfloat16* __restrict__ ax, const __hip_bfloat16* __restrict__ ak, const __hip_bfloat16* __restrict__ av,
          const __hip_bfloat16* __restrict__ awq, const __hip_bfloat16* __restrict__ awo,
          const __hip_bfloat16* __restrict__ bx, const __hip_bfloat16* __restrict__ bk, const __hip_bfloat16* __restrict__ bv,
          const __hip_bfloat16* __restrict__ bwq, const __hip_bfloat16* __restrict__ bwo,
          u32x4* ws, __hip_bfloat16* out
          ) {
  __shared__ __attribute__((aligned(16))) unsigned char lds[LDS_BYTES];
  LAS unsigned char* L8 = (LAS unsigned char*)lds;
  volatile LAS unsigned* ctl = (volatile LAS unsigned*)(L8 + CT_OFF);
  const int lane = threadIdx.x & 63, w = __builtin_amdgcn_readfirstlane(threadIdx.x >> 6);
  const int b = blockIdx.x, xcd = b & 7, bi = b >> 3;
  const bool isA = bi < 24;
  int h, j, T, team0, row0, n, sb, nb;
  if (isA) {
    h = 4 * xcd + bi / SPLIT_A; j = bi % SPLIT_A; T = SPLIT_A; team0 = h * SPLIT_A;
    row0 = (256 * j) / SPLIT_A; n = (256 * (j + 1)) / SPLIT_A - row0;
    sb = j < 4 ? 2 * j : j + 4; nb = j < 4 ? 2 : 1;
  } else {
    h = 4 * xcd + (bi - 24) / SPLIT_B; j = (bi - 24) % SPLIT_B; T = SPLIT_B; team0 = NWG_A + h * SPLIT_B;
    row0 = (SK - WIN) / 32 + 32 * j; n = 32;
    sb = 5 * j; nb = 5;
  }
  const int wg = team0 + j;
  u32x4* ysl = ws + Y0 + (size_t)((isA ? 0 : NH) + h) * (DM / 2) + 32 * sb;
  if (w < NLOAD) {
    const size_t off = ((size_t)h * (SK / 32) + row0) * ROWB + w * 4096 + lane * 16;
    const char* kp = (const char*)(isA ? ak : bk) + off;
    const char* vp = (const char*)(isA ? av : bv) + off - (size_t)n * ROWB;
    volatile LAS unsigned* full = ctl + 8 + w;
    const unsigned lbase = (unsigned)(size_t)L8 + w * 4096;
    asm volatile("s_barrier" ::: "memory");
#pragma unroll
    for (int R = 0; R < IR; ++R) dma4k(kp + (size_t)R * ROWB, lbase + R * ROWB);
    int slot = IR, lim = RR, pub = 0;
#pragma unroll 1
    for (int R = IR; R < 2 * n; ++R) {
      if (R >= lim) {
        bool drained = false;
        for (unsigned sp = 0; sp < SPIN_CAP; ++sp) {
          const u32x4 f = *(volatile LAS u32x4*)(ctl + 4 * w);
          lim = __builtin_amdgcn_readfirstlane(min(min(f[0], f[1]), min(f[2], f[3]))) + RR;
          if (R < lim) break;
          if (!drained) { vmw<0>(); *full = R; pub = R; drained = true; }
          __builtin_amdgcn_s_sleep(1);
        }
        asm volatile("" ::: "memory");
      }
      dma4k((R < n ? kp : vp) + (size_t)R * ROWB, lbase + slot * ROWB);
      slot = wrapr(slot + 1);
      vmw<4 * IR>();
      if (R - IR + 1 > pub) { pub = R - IR + 1; *full = pub; }
    }
    drain_ladder<IR - 1>(full, 2 * n, pub);
    return;
  }
  LAS float* qp = (LAS float*)(L8 + QP_OFF);
  if (w == NLOAD) {
    const u64 ep = ld_g(reinterpret_cast<const u64*>(ws));
    if (lane < 16) ctl[lane] = 0u;
    BAR();
    const u64 tag64 = ep + 1ull;
    const u32x2 tag = {(unsigned)tag64, (unsigned)(tag64 >> 32)};
    if (lane == 0) st_g(reinterpret_cast<u64*>(ws + FLAG0) + wg, tag64);
    if (lane < 2) ctl[12 + lane] = tag[lane];
    for (unsigned sp = 0; ctl[10] < (unsigned)NCONS && sp < SPIN_CAP; ++sp) __builtin_amdgcn_s_sleep(1);
    SB();
    {
      float s0 = 0.f, s1 = 0.f;
#pragma unroll
      for (int u = 0; u < NCONS; ++u) { s0 += qp[u * DH + 2 * lane]; s1 += qp[u * DH + 2 * lane + 1]; }
      st_g16(ws + GR0 + (size_t)GSTR * wg + lane, s0, s1, tag);
    }
    {
      const u32x4* g = ws + GR0 + (size_t)GSTR * team0 + lane;
      int o[6];
#pragma unroll
      for (int u = 0; u < SPLIT_A; ++u) o[u] = GSTR * min(u, T - 1);
      u32x4 v[6];
      for (unsigned sp = 0; sp < SPIN_CAP; ++sp) {
        ld_g16x6(g, o, v);
        bool ok = true;
#pragma unroll
        for (int u = 0; u < SPLIT_A; ++u) ok &= gok(v[u], tag);
        if (__all(ok)) break;
        __builtin_amdgcn_s_sleep(1);
      }
      float s0 = 0.f, s1 = 0.f;
#pragma unroll
      for (int u = 0; u < SPLIT_A; ++u) if (u < T) { s0 += __uint_as_float(v[u][0]); s1 += __uint_as_float(v[u][2]); }
      ((LAS unsigned*)(L8 + QF_OFF))[lane] = pack_bf16(s0, s1);
      LGKM0();
      if (lane == 0) ctl[11] = 1u;
    }
    if (b == 0) {
      for (unsigned sp = 0; sp < SPIN_CAP; ++sp) {
        bool ok = true;
#pragma unroll
        for (int k = 0; k < 4; ++k) ok &= ld_g(reinterpret_cast<const u64*>(ws + FLAG0) + 64 * k + lane) == tag64;
        if (__all(ok)) break;
        __builtin_amdgcn_s_sleep(8);
      }
      if (lane == 0) st_g(reinterpret_cast<u64*>(ws), tag64);
    }
    return;
  }
  const int ci = w - NLOAD - 1, ctid = ci * 64 + lane;
  const int r = lane >> 4, c = lane & 15, r8 = lane >> 3, c8 = lane & 7;
  STAMP(0);
  LAS float* ap = (LAS float*)(L8 + AP_OFF);
  LAS float* ml = (LAS float*)(L8 + ML_OFF);
  LAS float* yp = (LAS float*)L8;
  u32x4 wor[5][2];
  {
    const unsigned short* x = reinterpret_cast<const unsigned short*>(isA ? ax : bx);
    const __hip_bfloat16* wq = (isA ? awq : bwq) + h * DH + 8 * c;
    const int qr0 = (DM * j) / T, qcnt = (DM * (j + 1)) / T - qr0;
    auto prologue = [&](auto nq_, auto nb_) {
      constexpr int NQ = decltype(nq_)::value, NB = decltype(nb_)::value;
      u32x4 wr[NQ]; unsigned xs[NQ];
      asm volatile("; prologue %0" :: "n"(NQ) : "memory");
#pragma unroll
      for (int i = 0; i < NQ; ++i) {
        const int row = qr0 + min(32 * i + 4 * ci + r, qcnt - 1);
        wr[i] = ldw(reinterpret_cast<const u32x4*>(wq + (size_t)row * (NH * DH)));
        xs[i] = x[row];
      }
      SB();
      BAR();
      SB();
      float qa[8];
#pragma unroll
      for (int k = 0; k < 8; ++k) qa[k] = 0.f;
#pragma unroll
      for (int i = 0; i < NQ; ++i) {
        asm volatile("" : "+v"(wr[i]), "+v"(xs[i]));
        const float xv = (32 * i + 4 * ci + r < qcnt) ? __uint_as_float(xs[i] << 16) : 0.f;
#pragma unroll
        for (int k = 0; k < 4; ++k) {
          qa[2 * k] = fmaf(xv, bf_lo(wr[i][k]), qa[2 * k]);
          qa[2 * k + 1] = fmaf(xv, bf_hi(wr[i][k]), qa[2 * k + 1]);
        }
      }
#pragma unroll
      for (int k = 0; k < 8; ++k) qa[k] = xrow_sum(qa[k]);
      if (r == 0) {
        *reinterpret_cast<LAS f32x4*>(qp + ci * DH + 8 * c) = f32x4{qa[0], qa[1], qa[2], qa[3]};
        *reinterpret_cast<LAS f32x4*>(qp + ci * DH + 8 * c + 4) = f32x4{qa[4], qa[5], qa[6], qa[7]};
      }
      __builtin_amdgcn_s_waitcnt(0);
      SB();
      if (lane == 0) __hip_atomic_fetch_add((LAS unsigned*)(ctl + 10), 1u, __ATOMIC_RELAXED, __HIP_MEMORY_SCOPE_WORKGROUP);
      SB();
    };
    if (isA) prologue(std::integral_constant<int, 4>{}, std::integral_constant<int, 2>{});
    else prologue(std::integral_constant<int, 10>{}, std::integral_constant<int, 5>{});
    SB();
  }
  const int G = (n + 15) >> 4;
  volatile LAS unsigned* full = ctl + 8 + (ci >> 2);
  LAS unsigned char* mine = L8 + ci * 1024 + lane * 16;
  int F = 0, s0r = 0;
#define FETCH4(R0, base, d)                                                                     \
    const int cnt_ = min(4, n - (R0)), need_ = (base) + (R0) + cnt_;                            \
    for (unsigned sp = 0; F < need_ && sp < SPIN_CAP; ++sp) { F = __builtin_amdgcn_readfirstlane(*full); if (F < need_) __builtin_amdgcn_s_sleep(1); } \
    asm volatile("" ::: "memory");                                                              \
    _Pragma("unroll") for (int t = 0; t < 4; ++t)                                               \
      (d)[t] = *reinterpret_cast<const LAS u32x4*>(mine + wrapr(s0r + min(t, cnt_ - 1)) * ROWB);  \
    LGKM0();                                                                                    \
    if (lane == 0) ctl[ci] = need_;                                                             \
    asm volatile("" : "+v"((d)[0]), "+v"((d)[1]), "+v"((d)[2]), "+v"((d)[3]) :: "memory");      \
    s0r = wrapr(s0r + cnt_);
  u32x4 q = {0u, 0u, 0u, 0u};
  float sc0 = -INFINITY, sc1 = -INFINITY, sc2 = -INFINITY;
#pragma unroll 1
  for (int g = 0; g < G; ++g) {
    u32x4 kd[16];
#pragma unroll
    for (int bb = 0; bb < 4; ++bb) {
      const int R0 = 16 * g + 4 * bb;
      if (R0 < n) { FETCH4(R0, 0, kd + 4 * bb) }
      else {
#pragma unroll
        for (int t = 0; t < 4; ++t) kd[4 * bb + t] = u32x4{0u, 0u, 0u, 0u};
      }
    }
    if (g == 0) {
      STAMP(1);
      for (unsigned sp = 0; ctl[11] == 0u && sp < SPIN_CAP; ++sp) __builtin_amdgcn_s_sleep(1);
      SB();
      q = *reinterpret_cast<const LAS u32x4*>(L8 + QF_OFF + 16 * c);
      STAMP(2);
    }
    float xx[16];
#pragma unroll
    for (int t = 0; t < 16; ++t) {
      float a = 0.f;
#pragma unroll
      for (int k = 0; k < 4; ++k) a = dot2(kd[t][k], q[k], a);
      xx[t] = a;
    }
    float v = transpose_reduce16(xx, c) * SC2;
    v = (16 * g + c < n) ? v : -INFINITY;
    sc2 = sc1; sc1 = sc0; sc0 = v;
  }
  STAMP(3);
  const float m = wave_max(fmaxf(fmaxf(sc0, sc1), sc2));
  const float p0 = __builtin_amdgcn_exp2f(sc0 - m), p1 = __builtin_amdgcn_exp2f(sc1 - m), p2 = __builtin_amdgcn_exp2f(sc2 - m);
  const float l = wave_sum(p0 + p1 + p2);
  const __hip_bfloat16* wop = (isA ? awo : bwo) + (size_t)(h * DH + 16 * ci + r8) * DM + 64 * sb + 8 * c8;
#pragma unroll
  for (int k = 0; k < 5; ++k) asm volatile("" : "=v"(wor[k][0]), "=v"(wor[k][1]));
#define WOLOAD(NB) do { _Pragma("unroll") for (int k = 0; k < (NB); ++k) { const int kk = min(k, nb - 1);     \
      wor[k][0] = ldw(reinterpret_cast<const u32x4*>(wop + 64 * kk));                                          \
      wor[k][1] = ldw(reinterpret_cast<const u32x4*>(wop + (size_t)8 * DM + 64 * kk)); } SB(); } while (0)
  float acc[8];
#pragma unroll
  for (int k = 0; k < 8; ++k) acc[k] = 0.f;
#pragma unroll 1
  for (int g = 0; g < G; ++g) {
    const int back = G - 1 - g;
    const float p = back == 0 ? p0 : back == 1 ? p1 : p2;
#define PVB(BB) { const int R0 = 16 * g + 4 * (BB); if (R0 < n) { u32x4 d[4]; FETCH4(R0, n, d)  \
      pv_step<4 * (BB)>(d[0], p, acc); pv_step<4 * (BB) + 1>(d[1], p, acc); pv_step<4 * (BB) + 2>(d[2], p, acc); pv_step<4 * (BB) + 3>(d[3], p, acc); } }
    PVB(0) PVB(1)
    if (isA && g == WOG_A) WOLOAD(2);
    PVB(2)
    if (!isA && g == 1) WOLOAD(5);
    PVB(3)
#undef PVB
  }
  STAMP(4);
#pragma unroll
  for (int k = 0; k < 8; ++k) acc[k] = xrow_sum(acc[k]);
  if (r == 0) {
    *reinterpret_cast<LAS f32x4*>(ap + ci * DH + 8 * c) = f32x4{acc[0], acc[1], acc[2], acc[3]};
    *reinterpret_cast<LAS f32x4*>(ap + ci * DH + 8 * c + 4) = f32x4{acc[4], acc[5], acc[6], acc[7]};
    if (c == 0) { ml[2 * ci] = m; ml[2 * ci + 1] = l; }
  }
  BAR();
  float of;
  {
    float M = -INFINITY, L = 0.f, o = 0.f;
#pragma unroll
    for (int u = 0; u < NCONS; ++u) M = fmaxf(M, ml[2 * u]);
#pragma unroll
    for (int u = 0; u < NCONS; ++u) {
      const float sc = __builtin_amdgcn_exp2f(ml[2 * u] - M);
      L = fmaf(sc, ml[2 * u + 1], L);
      o = fmaf(sc, ap[u * DH + 16 * ci + c], o);
    }
    for (unsigned sp = 0; ctl[11] == 0u && sp < SPIN_CAP; ++sp) __builtin_amdgcn_s_sleep(1);
    const u32x2 tag = {ctl[12], ctl[13]};
    u32x4* gr = ws + GR0 + (size_t)GSTR * wg + 64;
    const float on = dpp<XOR1>(o);
    if (lane < 16 && !(lane & 1)) st_g16(gr + 8 * ci + (lane >> 1), o, on, tag);
    else if (ci == 0 && lane == 16) st_g16(gr + 64, M, L, tag);
    STAMP(5);
    const bool a0 = r < T, a1 = r + 4 < T;
    const u32x4* g0 = ws + GR0 + (size_t)GSTR * (team0 + (a0 ? r : 0)) + 64;
    const u32x4* g1 = ws + GR0 + (size_t)GSTR * (team0 + (a1 ? r + 4 : 0)) + 64;
    u32x4 vo0, vm0, vo1, vm1;
    for (unsigned sp = 0; sp < SPIN_CAP; ++sp) {
      ld_g16x4(g0 + 8 * ci + (c >> 1), g0 + 64, g1 + 8 * ci + (c >> 1), g1 + 64, vo0, vm0, vo1, vm1);
      if (__all(gok(vo0, tag) && gok(vm0, tag) && gok(vo1, tag) && gok(vm1, tag))) break;
      __builtin_amdgcn_s_sleep(PS1);
    }
    const float m0 = a0 ? __uint_as_float(vm0[0]) : -INFINITY, m1 = a1 ? __uint_as_float(vm1[0]) : -INFINITY;
    const float Mh = xrow_max(fmaxf(m0, m1));
    const float e0 = __builtin_amdgcn_exp2f(m0 - Mh), e1 = __builtin_amdgcn_exp2f(m1 - Mh);
    const float Lh = xrow_sum(fmaf(e0, __uint_as_float(vm0[2]), e1 * __uint_as_float(vm1[2])));
    const float x0 = __uint_as_float((c & 1) ? vo0[2] : vo0[0]), x1 = __uint_as_float((c & 1) ? vo1[2] : vo1[0]);
    of = xrow_sum(fmaf(e0, x0, e1 * x1)) / Lh;
  }
  STAMP(6);
  {
    const float o0 = __shfl(of, r8), o1 = __shfl(of, 8 + r8);
#pragma unroll
    for (int k = 0; k < 5; ++k) {
      if (k < nb) {
        float y[8];
#pragma unroll
        for (int e = 0; e < 4; ++e) {
          y[2 * e] = fmaf(o0, bf_lo(wor[k][0][e]), o1 * bf_lo(wor[k][1][e]));
          y[2 * e + 1] = fmaf(o0, bf_hi(wor[k][0][e]), o1 * bf_hi(wor[k][1][e]));
        }
#pragma unroll
        for (int e = 0; e < 8; ++e) { y[e] += dpp<XOR8>(y[e]); y[e] = xrow_sum(y[e]); }
        if (r8 == 0) {
          *reinterpret_cast<LAS f32x4*>(yp + ci * 320 + 64 * k + 8 * c8) = f32x4{y[0], y[1], y[2], y[3]};
          *reinterpret_cast<LAS f32x4*>(yp + ci * 320 + 64 * k + 8 * c8 + 4) = f32x4{y[4], y[5], y[6], y[7]};
        }
      }
    }
  }
  BAR();
  const u32x2 tag = {ctl[12], ctl[13]};
  if (ctid < 32 * nb) {
    float s0 = 0.f, s1 = 0.f;
#pragma unroll
    for (int u = 0; u < NCONS; ++u) { s0 += yp[u * 320 + 2 * ctid]; s1 += yp[u * 320 + 2 * ctid + 1]; }
    st_g16(ysl + ctid, s0, s1, tag);
  }
  if (ci == NCONS - 1 && b < 160) {
    const int br = b / 80, col0 = 8 * (b % 80);
    const u32x4* g = ws + Y0 + (size_t)(NH * br + (lane >> 2)) * (DM / 2) + col0 / 2 + (lane & 3);
    u32x4 v0, v1;
    for (unsigned sp = 0; sp < SPIN_CAP; ++sp) {
      ld_g16x2(g, g + (size_t)16 * (DM / 2), v0, v1);
      if (__all(gok(v0, tag) && gok(v1, tag))) break;
      __builtin_amdgcn_s_sleep(PS2);
    }
    float s0 = __uint_as_float(v0[0]) + __uint_as_float(v1[0]), s1 = __uint_as_float(v0[2]) + __uint_as_float(v1[2]);
    s0 += dpp<0x124>(s0); s1 += dpp<0x124>(s1);
    s0 += dpp<XOR8>(s0); s1 += dpp<XOR8>(s1);
    s0 = xrow_sum(s0); s1 = xrow_sum(s1);
    if (lane < 4) reinterpret_cast<unsigned*>(out + br * DM + col0)[lane] = pack_bf16(s0, s1);
  }
  STAMP(7);
}

extern "C" void kernel_launch(void* const* d_in, const int* in_sizes, int n_in,
                              void* d_out, int out_size, void* d_ws, size_t ws_size,
                              hipStream_t stream) {
  typedef const __hip_bfloat16* P;
#define XA
  attn_main<<<NWG, NTHR, 0, stream>>>((P)d_in[0], (P)d_in[1], (P)d_in[2], (P)d_in[3], (P)d_in[4],
                                      (P)d_in[5], (P)d_in[6], (P)d_in[7], (P)d_in[8], (P)d_in[9],
                                      (u32x4*)d_ws, (__hip_bfloat16*)d_out XA);
}
```

```cpp
#include <hip/hip_runtime.h>
#include <hip/hip_bf16.h>
#include <stdint.h>
#include <type_traits>

typedef unsigned u32x4 __attribute__((ext_vector_type(4)));
typedef unsigned u32x2 __attribute__((ext_vector_type(2)));
typedef float f32x4 __attribute__((ext_vector_type(4)));
typedef __bf16 bf16x2 __attribute__((ext_vector_type(2)));
#define LAS __attribute__((address_space(3)))

constexpr int DM = 640, NH = 32, DH = 128, SK = 8192, WIN = 2048;
typedef unsigned long long u64;
constexpr int NCONS = 8, NLOAD = 2, NTHR = (NCONS + NLOAD + 1) * 64;
constexpr int SPLIT_A = 6, SPLIT_B = 2;
constexpr int NWG_A = NH * SPLIT_A, NWG_B = NH * SPLIT_B, NWG = NWG_A + NWG_B;
constexpr float SC2 = 0.088388347648318440f * 1.4426950408889634f;
#define RR 18
#define IR 5
#define WNT 1
constexpr int ROWB = 8192;
constexpr unsigned SPIN_CAP = 1u << 20;
constexpr int QP_OFF = RR * ROWB, AP_OFF = QP_OFF + 4096, ML_OFF = AP_OFF + 4096, QF_OFF = ML_OFF + 64, CT_OFF = QF_OFF + 256, LDS_BYTES = CT_OFF + 64;
static_assert(LDS_BYTES <= 163840 && RR * ROWB >= NCONS * 320 * 4, "LDS");
constexpr int FLAG0 = 64, GR0 = 512, GSTR = 136, Y0 = GR0 + GSTR * NWG;

#define SB() do { asm volatile("" ::: "memory"); __builtin_amdgcn_sched_barrier(0); } while (0)
#define PIN(x) asm volatile("" : "+v"(x))
#define LGKM0() asm volatile("s_waitcnt lgkmcnt(0)" ::: "memory")
#define BAR() asm volatile("s_waitcnt lgkmcnt(0)\n\ts_barrier" ::: "memory")
#define STAMP(i)

__device__ __forceinline__ float dot2(unsigned k, unsigned q, float acc) {
  return __builtin_amdgcn_fdot2_f32_bf16(__builtin_bit_cast(bf16x2, k), __builtin_bit_cast(bf16x2, q), acc, false);
}
template <int CTRL> __device__ __forceinline__ float dpp(float x) {
  return __builtin_bit_cast(float, __builtin_amdgcn_mov_dpp(__builtin_bit_cast(int, x), CTRL, 0xf, 0xf, true));
}
constexpr int XOR1 = 0xB1, XOR2 = 0x4E, XOR7 = 0x141, XOR8 = 0x128;
template <int T> __device__ __forceinline__ float bcast16(float p) { return dpp<0x150 + T>(p); }
__device__ __forceinline__ float xrow_max(float x) {
  auto s = __builtin_amdgcn_permlane16_swap(__float_as_uint(x), __float_as_uint(x), false, false);
  x = fmaxf(__uint_as_float(s[0]), __uint_as_float(s[1]));
  auto t = __builtin_amdgcn_permlane32_swap(__float_as_uint(x), __float_as_uint(x), false, false);
  return fmaxf(__uint_as_float(t[0]), __uint_as_float(t[1]));
}
__device__ __forceinline__ float xrow_sum(float x) {
  auto s = __builtin_amdgcn_permlane16_swap(__float_as_uint(x), __float_as_uint(x), false, false);
  x = __uint_as_float(s[0]) + __uint_as_float(s[1]);
  auto t = __builtin_amdgcn_permlane32_swap(__float_as_uint(x), __float_as_uint(x), false, false);
  return __uint_as_float(t[0]) + __uint_as_float(t[1]);
}
__device__ __forceinline__ float wave_max(float x) {
  x = fmaxf(x, dpp<XOR1>(x)); x = fmaxf(x, dpp<XOR2>(x)); x = fmaxf(x, dpp<XOR7>(x)); x = fmaxf(x, dpp<XOR8>(x));
  return xrow_max(x);
}
__device__ __forceinline__ float wave_sum(float x) {
  x += dpp<XOR1>(x); x += dpp<XOR2>(x); x += dpp<XOR7>(x); x += dpp<XOR8>(x);
  return xrow_sum(x);
}
__device__ __forceinline__ float bf_lo(unsigned u) { return __uint_as_float(u << 16); }
__device__ __forceinline__ float bf_hi(unsigned u) { return __uint_as_float(u & 0xffff0000u); }
__device__ __forceinline__ unsigned pack_bf16(float lo, float hi) {
  unsigned r; asm("v_cvt_pk_bf16_f32 %0, %1, %2" : "=v"(r) : "v"(lo), "v"(hi)); return r;
}
__device__ __forceinline__ u64 ld_g(const u64* p) { return __hip_atomic_load(p, __ATOMIC_RELAXED, __HIP_MEMORY_SCOPE_AGENT); }
__device__ __forceinline__ void st_g(u64* p, u64 v) { __hip_atomic_store(p, v, __ATOMIC_RELAXED, __HIP_MEMORY_SCOPE_AGENT); }
__device__ __forceinline__ void st_g16(u32x4* p, float v0, float v1, u32x2 tag) {
  const u32x4 v = {__float_as_uint(v0), tag[0], __float_as_uint(v1), tag[1]};
  asm volatile("global_store_dwordx4 %0, %1, off sc1\n\ts_nop 1" :: "v"(p), "v"(v) : "memory");
}
__device__ __forceinline__ void ld_g16x2(const u32x4* p0, const u32x4* p1, u32x4& a, u32x4& b) {
  asm volatile("global_load_dwordx4 %0, %2, off sc1\n\tglobal_load_dwordx4 %1, %3, off sc1\n\ts_waitcnt vmcnt(0)"
               : "=&v"(a), "=&v"(b) : "v"(p0), "v"(p1) : "memory");
}
__device__ __forceinline__ void ld_g16x4(const u32x4* p0, const u32x4* p1, const u32x4* p2, const u32x4* p3, u32x4& a, u32x4& b, u32x4& c, u32x4& d) {
  asm volatile("global_load_dwordx4 %0, %4, off sc1\n\tglobal_load_dwordx4 %1, %5, off sc1\n\t"
               "global_load_dwordx4 %2, %6, off sc1\n\tglobal_load_dwordx4 %3, %7, off sc1\n\ts_waitcnt vmcnt(0)"
               : "=&v"(a), "=&v"(b), "=&v"(c), "=&v"(d) : "v"(p0), "v"(p1), "v"(p2), "v"(p3) : "memory");
}
__device__ __forceinline__ void ld_g16x6(const u32x4* p, const int (&o)[6], u32x4 (&v)[6]) {
  asm volatile("global_load_dwordx4 %0, %6, off sc1\n\tglobal_load_dwordx4 %1, %7, off sc1\n\t"
               "global_load_dwordx4 %2, %8, off sc1\n\tglobal_load_dwordx4 %3, %9, off sc1\n\t"
               "global_load_dwordx4 %4, %10, off sc1\n\tglobal_load_dwordx4 %5, %11, off sc1\n\ts_waitcnt vmcnt(0)"
               : "=&v"(v[0]), "=&v"(v[1]), "=&v"(v[2]), "=&v"(v[3]), "=&v"(v[4]), "=&v"(v[5])
               : "v"(p + o[0]), "v"(p + o[1]), "v"(p + o[2]), "v"(p + o[3]), "v"(p + o[4]), "v"(p + o[5]) : "memory");
}
__device__ __forceinline__ bool gok(const u32x4 g, u32x2 tag) { return g[1] == tag[0] && g[3] == tag[1]; }
__device__ __forceinline__ u32x4 ldw(const u32x4* p) { return WNT ? __builtin_nontemporal_load(p) : *p; }

__device__ __forceinline__ float transpose_reduce16(float (&x)[16], int c) {
#define TR_STEP(HALF, CTRL, BIT)                                                          \
  _Pragma("unroll") for (int h = 0; h < (HALF); ++h) {                                    \
    const float lo_ = x[h], hi_ = x[h + (HALF)];                                          \
    const float t1_ = lo_ + dpp<CTRL>(lo_), t2_ = hi_ + dpp<CTRL>(hi_);                   \
    x[h] = (c & (BIT)) ? t2_ : t1_; }
  TR_STEP(8, XOR8, 8) TR_STEP(4, XOR7, 4) TR_STEP(2, XOR2, 2) TR_STEP(1, XOR1, 1)
#undef TR_STEP
  return x[0];
}
template <int T>
__device__ __forceinline__ void pv_step(const u32x4 d, float p, float (&acc)[8]) {
  const float pt = bcast16<T>(p);
#pragma unroll
  for (int k = 0; k < 4; ++k) {
    acc[2 * k] = fmaf(pt, bf_lo(d[k]), acc[2 * k]);
    acc[2 * k + 1] = fmaf(pt, bf_hi(d[k]), acc[2 * k + 1]);
  }
}

__device__ __forceinline__ void dma4k(const void* gsrc, unsigned lds_dst) {
  unsigned keep;
  asm volatile("s_mov_b32 %0, m0\n\ts_mov_b32 m0, %2\n\ts_nop 0\n\t"
               "global_load_lds_dwordx4 %1, off nt\n\t"
               "global_load_lds_dwordx4 %1, off offset:1024 nt\n\t"
               "global_load_lds_dwordx4 %1, off offset:2048 nt\n\t"
               "global_load_lds_dwordx4 %1, off offset:3072 nt\n\t"
               "s_mov_b32 m0, %0" : "=&s"(keep) : "v"(gsrc), "s"(lds_dst) : "memory");
}
template <int N> __device__ __forceinline__ void vmw() { asm volatile("s_waitcnt vmcnt(%0)" :: "n"(N) : "memory"); }
template <int K> __device__ __forceinline__ void drain_ladder(volatile LAS unsigned* full, int total, int pub) {
  if constexpr (K >= 0) { vmw<4 * K>(); if (total - K > pub) *full = total - K; drain_ladder<K - 1>(full, total, pub); }
}
__device__ __forceinline__ int wrapr(int s) { return s >= RR ? s - RR : s; }

__global__ void __launch_bounds__(NTHR)
attn_main(const __hip_bfloat16* __restrict__ ax, const __hip_bfloat16* __restrict__ ak, const __hip_bfloat16* __restrict__ av,
          const __hip_bfloat16* __restrict__ awq, const __hip_bfloat16* __restrict__ awo,
          const __hip_bfloat16* __restrict__ bx, const __hip_bfloat16* __restrict__ bk, const __hip_bfloat16* __restrict__ bv,
          const __hip_bfloat16* __restrict__ bwq, const __hip_bfloat16* __restrict__ bwo,
          u32x4* ws, __hip_bfloat16* out
          ) {
  __shared__ __attribute__((aligned(16))) unsigned char lds[LDS_BYTES];
  LAS unsigned char* L8 = (LAS unsigned char*)lds;
  volatile LAS unsigned* ctl = (volatile LAS unsigned*)(L8 + CT_OFF);
  const int lane = threadIdx.x & 63, w = __builtin_amdgcn_readfirstlane(threadIdx.x >> 6);
  const int b = blockIdx.x, xcd = b & 7, bi = b >> 3;
  const bool isA = bi < 24;
  int h, j, T, team0, row0, n, sb, nb;
  if (isA) {
    h = 4 * xcd + bi / SPLIT_A; j = bi % SPLIT_A; T = SPLIT_A; team0 = h * SPLIT_A;
    row0 = (256 * j) / SPLIT_A; n = (256 * (j + 1)) / SPLIT_A - row0;
    sb = j < 4 ? 2 * j : j + 4; nb = j < 4 ? 2 : 1;
  } else {
    h = 4 * xcd + (bi - 24) / SPLIT_B; j = (bi - 24) % SPLIT_B; T = SPLIT_B; team0 = NWG_A + h * SPLIT_B;
    row0 = (SK - WIN) / 32 + 32 * j; n = 32;
    sb = 5 * j; nb = 5;
  }
  const int wg = team0 + j;
  u32x4* ysl = ws + Y0 + (size_t)((isA ? 0 : NH) + h) * (DM / 2) + 32 * sb;
  if (w < NLOAD) {
    const size_t off = ((size_t)h * (SK / 32) + row0) * ROWB + w * 4096 + lane * 16;
    const char* kp = (const char*)(isA ? ak : bk) + off;
    const char* vp = (const char*)(isA ? av : bv) + off - (size_t)n * ROWB;
    volatile LAS unsigned* full = ctl + 8 + w;
    const unsigned lbase = (unsigned)(size_t)L8 + w * 4096;
    asm volatile("s_barrier" ::: "memory");
#pragma unroll
    for (int R = 0; R < IR; ++R) dma4k(kp + (size_t)R * ROWB, lbase + R * ROWB);
    int slot = IR, lim = RR, pub = 0;
#pragma unroll 1
    for (int R = IR; R < 2 * n; ++R) {
      if (R >= lim) {
        bool drained = false;
        for (unsigned sp = 0; sp < SPIN_CAP; ++sp) {
          const u32x4 f = *(volatile LAS u32x4*)(ctl + 4 * w);
          lim = __builtin_amdgcn_readfirstlane(min(min(f[0], f[1]), min(f[2], f[3]))) + RR;
          if (R < lim) break;
          if (!drained) { vmw<0>(); *full = R; pub = R; drained = true; }
          __builtin_amdgcn_s_sleep(1);
        }
        asm volatile("" ::: "memory");
      }
      dma4k((R < n ? kp : vp) + (size_t)R * ROWB, lbase + slot * ROWB);
      slot = wrapr(slot + 1);
      vmw<4 * IR>();
      if (R - IR + 1 > pub) { pub = R - IR + 1; *full = pub; }
    }
    drain_ladder<IR - 1>(full, 2 * n, pub);
    return;
  }
  LAS float* qp = (LAS float*)(L8 + QP_OFF);
  if (w == NLOAD) {
    const u64 ep = ld_g(reinterpret_cast<const u64*>(ws));
    if (lane < 16) ctl[lane] = 0u;
    BAR();
    const u64 tag64 = ep + 1ull;
    const u32x2 tag = {(unsigned)tag64, (unsigned)(tag64 >> 32)};
    if (lane == 0) st_g(reinterpret_cast<u64*>(ws + FLAG0) + wg, tag64);
    if (lane < 2) ctl[12 + lane] = tag[lane];
    for (unsigned sp = 0; ctl[10] < (unsigned)NCONS && sp < SPIN_CAP; ++sp) __builtin_amdgcn_s_sleep(1);
    SB();
    {
      float s0 = 0.f, s1 = 0.f;
#pragma unroll
      for (int u = 0; u < NCONS; ++u) { s0 += qp[u * DH + 2 * lane]; s1 += qp[u * DH + 2 * lane + 1]; }
      st_g16(ws + GR0 + (size_t)GSTR * wg + lane, s0, s1, tag);
    }
    {
      const u32x4* g = ws + GR0 + (size_t)GSTR * team0 + lane;
      int o[6];
#pragma unroll
      for (int u = 0; u < SPLIT_A; ++u) o[u] = GSTR * min(u, T - 1);
      u32x4 v[6];
      for (unsigned sp = 0; sp < SPIN_CAP; ++sp) {
        ld_g16x6(g, o, v);
        bool ok = true;
#pragma unroll
        for (int u = 0; u < SPLIT_A; ++u) ok &= gok(v[u], tag);
        if (__all(ok)) break;
        __builtin_amdgcn_s_sleep(1);
      }
      float s0 = 0.f, s1 = 0.f;
#pragma unroll
      for (int u = 0; u < SPLIT_A; ++u) if (u < T) { s0 += __uint_as_float(v[u][0]); s1 += __uint_as_float(v[u][2]); }
      ((LAS unsigned*)(L8 + QF_OFF))[lane] = pack_bf16(s0, s1);
      LGKM0();
      if (lane == 0) ctl[11] = 1u;
    }
    if (b == 0) {
      for (unsigned sp = 0; sp < SPIN_CAP; ++sp) {
        bool ok = true;
#pragma unroll
        for (int k = 0; k < 4; ++k) ok &= ld_g(reinterpret_cast<const u64*>(ws + FLAG0) + 64 * k + lane) == tag64;
        if (__all(ok)) break;
        __builtin_amdgcn_s_sleep(8);
      }
      if (lane == 0) st_g(reinterpret_cast<u64*>(ws), tag64);
    }
    return;
  }
  const int ci = w - NLOAD - 1, ctid = ci * 64 + lane;
  const int r = lane >> 4, c = lane & 15, r8 = lane >> 3, c8 = lane & 7;
  STAMP(0);
  LAS float* ap = (LAS float*)(L8 + AP_OFF);
  LAS float* ml = (LAS float*)(L8 + ML_OFF);
  LAS float* yp = (LAS float*)L8;
  u32x4 wor[5][2];
  {
    const unsigned short* x = reinterpret_cast<const unsigned short*>(isA ? ax : bx);
    const __hip_bfloat16* wq = (isA ? awq : bwq) + h * DH + 8 * c;
    const __hip_bfloat16* wop = (isA ? awo : bwo) + (size_t)(h * DH + 16 * ci + r8) * DM + 64 * sb + 8 * c8;
    const int qr0 = (DM * j) / T, qcnt = (DM * (j + 1)) / T - qr0;
    auto prologue = [&](auto nq_, auto nb_) {
      constexpr int NQ = decltype(nq_)::value, NB = decltype(nb_)::value;
      u32x4 wr[NQ]; unsigned xs[NQ];
      asm volatile("; prologue %0" :: "n"(NQ) : "memory");
#pragma unroll
      for (int i = 0; i < NQ; ++i) {
        const int row = qr0 + min(32 * i + 4 * ci + r, qcnt - 1);
        wr[i] = ldw(reinterpret_cast<const u32x4*>(wq + (size_t)row * (NH * DH)));
        xs[i] = x[row];
      }
      SB();
      BAR();
      SB();
      float qa[8];
#pragma unroll
      for (int k = 0; k < 8; ++k) qa[k] = 0.f;
#pragma unroll
      for (int i = 0; i < NQ; ++i) {
        asm volatile("" : "+v"(wr[i]), "+v"(xs[i]));
        const float xv = (32 * i + 4 * ci + r < qcnt) ? __uint_as_float(xs[i] << 16) : 0.f;
#pragma unroll
        for (int k = 0; k < 4; ++k) {
          qa[2 * k] = fmaf(xv, bf_lo(wr[i][k]), qa[2 * k]);
          qa[2 * k + 1] = fmaf(xv, bf_hi(wr[i][k]), qa[2 * k + 1]);
        }
      }
#pragma unroll
      for (int k = 0; k < 8; ++k) qa[k] = xrow_sum(qa[k]);
      if (r == 0) {
        *reinterpret_cast<LAS f32x4*>(qp + ci * DH + 8 * c) = f32x4{qa[0], qa[1], qa[2], qa[3]};
        *reinterpret_cast<LAS f32x4*>(qp + ci * DH + 8 * c + 4) = f32x4{qa[4], qa[5], qa[6], qa[7]};
      }
      __builtin_amdgcn_s_waitcnt(0);
      SB();
      if (lane == 0) __hip_atomic_fetch_add((LAS unsigned*)(ctl + 10), 1u, __ATOMIC_RELAXED, __HIP_MEMORY_SCOPE_WORKGROUP);
      SB();
#pragma unroll
      for (int k = 0; k < 5; ++k) {
        if (k < NB) {
          const int kk = min(k, nb - 1);
          wor[k][0] = ldw(reinterpret_cast<const u32x4*>(wop + 64 * kk));
          wor[k][1] = ldw(reinterpret_cast<const u32x4*>(wop + (size_t)8 * DM + 64 * kk));
        } else { asm volatile("" : "=v"(wor[k][0]), "=v"(wor[k][1])); }
      }
    };
    if (isA) prologue(std::integral_constant<int, 4>{}, std::integral_constant<int, 2>{});
    else prologue(std::integral_constant<int, 10>{}, std::integral_constant<int, 5>{});
    SB();
  }
  STAMP(1);
  const int G = (n + 15) >> 4;
  volatile LAS unsigned* full = ctl + 8 + (ci >> 2);
  LAS unsigned char* mine = L8 + ci * 1024 + lane * 16;
  int F = 0, s0r = 0;
#define FETCH4(R0, base, d)                                                                     \
    const int cnt_ = min(4, n - (R0)), need_ = (base) + (R0) + cnt_;                            \
    for (unsigned sp = 0; F < need_ && sp < SPIN_CAP; ++sp) { F = __builtin_amdgcn_readfirstlane(*full); if (F < need_) __builtin_amdgcn_s_sleep(1); } \
    asm volatile("" ::: "memory");                                                              \
    _Pragma("unroll") for (int t = 0; t < 4; ++t)                                               \
      (d)[t] = *reinterpret_cast<const LAS u32x4*>(mine + wrapr(s0r + min(t, cnt_ - 1)) * ROWB);  \
    LGKM0();                                                                                    \
    if (lane == 0) ctl[ci] = need_;                                                             \
    asm volatile("" : "+v"((d)[0]), "+v"((d)[1]), "+v"((d)[2]), "+v"((d)[3]) :: "memory");      \
    s0r = wrapr(s0r + cnt_);
  u32x4 q = {0u, 0u, 0u, 0u};
  float sc0 = -INFINITY, sc1 = -INFINITY, sc2 = -INFINITY;
#pragma unroll 1
  for (int g = 0; g < G; ++g) {
    u32x4 kd[16];
#pragma unroll
    for (int bb = 0; bb < 4; ++bb) {
      const int R0 = 16 * g + 4 * bb;
      if (R0 < n) { FETCH4(R0, 0, kd + 4 * bb) }
      else {
#pragma unroll
        for (int t = 0; t < 4; ++t) kd[4 * bb + t] = u32x4{0u, 0u, 0u, 0u};
      }
    }
    if (g == 0) {
      for (unsigned sp = 0; ctl[11] == 0u && sp < SPIN_CAP; ++sp) __builtin_amdgcn_s_sleep(1);
      SB();
      q = *reinterpret_cast<const LAS u32x4*>(L8 + QF_OFF + 16 * c);
      STAMP(2);
    }
    float xx[16];
#pragma unroll
    for (int t = 0; t < 16; ++t) {
      float a = 0.f;
#pragma unroll
      for (int k = 0; k < 4; ++k) a = dot2(kd[t][k], q[k], a);
      xx[t] = a;
    }
    float v = transpose_reduce16(xx, c) * SC2;
    v = (16 * g + c < n) ? v : -INFINITY;
    sc2 = sc1; sc1 = sc0; sc0 = v;
  }
  STAMP(3);
  const float m = wave_max(fmaxf(fmaxf(sc0, sc1), sc2));
  const float p0 = __builtin_amdgcn_exp2f(sc0 - m), p1 = __builtin_amdgcn_exp2f(sc1 - m), p2 = __builtin_amdgcn_exp2f(sc2 - m);
  const float l = wave_sum(p0 + p1 + p2);
  float acc[8];
#pragma unroll
  for (int k = 0; k < 8; ++k) acc[k] = 0.f;
#pragma unroll 1
  for (int g = 0; g < G; ++g) {
    const int back = G - 1 - g;
    const float p = back == 0 ? p0 : back == 1 ? p1 : p2;
#define PVB(BB) { const int R0 = 16 * g + 4 * (BB); if (R0 < n) { u32x4 d[4]; FETCH4(R0, n, d)  \
      pv_step<4 * (BB)>(d[0], p, acc); pv_step<4 * (BB) + 1>(d[1], p, acc); pv_step<4 * (BB) + 2>(d[2], p, acc); pv_step<4 * (BB) + 3>(d[3], p, acc); } }
    PVB(0) PVB(1) PVB(2) PVB(3)
#undef PVB
  }
  STAMP(4);
#pragma unroll
  for (int k = 0; k < 8; ++k) acc[k] = xrow_sum(acc[k]);
  if (r == 0) {
    *reinterpret_cast<LAS f32x4*>(ap + ci * DH + 8 * c) = f32x4{acc[0], acc[1], acc[2], acc[3]};
    *reinterpret_cast<LAS f32x4*>(ap + ci * DH + 8 * c + 4) = f32x4{acc[4], acc[5], acc[6], acc[7]};
    if (c == 0) { ml[2 * ci] = m; ml[2 * ci + 1] = l; }
  }
  BAR();
  float of;
  {
    float M = -INFINITY, L = 0.f, o = 0.f;
#pragma unroll
    for (int u = 0; u < NCONS; ++u) M = fmaxf(M, ml[2 * u]);
#pragma unroll
    for (int u = 0; u < NCONS; ++u) {
      const float sc = __builtin_amdgcn_exp2f(ml[2 * u] - M);
      L = fmaf(sc, ml[2 * u + 1], L);
      o = fmaf(sc, ap[u * DH + 16 * ci + c], o);
    }
    const u32x2 tag = {ctl[12], ctl[13]};
    u32x4* gr = ws + GR0 + (size_t)GSTR * wg + 64;
    const float on = dpp<XOR1>(o);
    if (lane < 16 && !(lane & 1)) st_g16(gr + 8 * ci + (lane >> 1), o, on, tag);
    else if (ci == 0 && lane == 16) st_g16(gr + 64, M, L, tag);
    STAMP(5);
    const bool a0 = r < T, a1 = r + 4 < T;
    const u32x4* g0 = ws + GR0 + (size_t)GSTR * (team0 + (a0 ? r : 0)) + 64;
    const u32x4* g1 = ws + GR0 + (size_t)GSTR * (team0 + (a1 ? r + 4 : 0)) + 64;
    u32x4 vo0, vm0, vo1, vm1;
    for (unsigned sp = 0; sp < SPIN_CAP; ++sp) {
      ld_g16x4(g0 + 8 * ci + (c >> 1), g0 + 64, g1 + 8 * ci + (c >> 1), g1 + 64, vo0, vm0, vo1, vm1);
      if (__all(gok(vo0, tag) && gok(vm0, tag) && gok(vo1, tag) && gok(vm1, tag))) break;
      __builtin_amdgcn_s_sleep(1);
    }
    const float m0 = a0 ? __uint_as_float(vm0[0]) : -INFINITY, m1 = a1 ? __uint_as_float(vm1[0]) : -INFINITY;
    const float Mh = xrow_max(fmaxf(m0, m1));
    const float e0 = __builtin_amdgcn_exp2f(m0 - Mh), e1 = __builtin_amdgcn_exp2f(m1 - Mh);
    const float Lh = xrow_sum(fmaf(e0, __uint_as_float(vm0[2]), e1 * __uint_as_float(vm1[2])));
    const float x0 = __uint_as_float((c & 1) ? vo0[2] : vo0[0]), x1 = __uint_as_float((c & 1) ? vo1[2] : vo1[0]);
    of = xrow_sum(fmaf(e0, x0, e1 * x1)) / Lh;
  }
  STAMP(6);
  {
    const float o0 = __shfl(of, r8), o1 = __shfl(of, 8 + r8);
#pragma unroll
    for (int k = 0; k < 5; ++k) {
      if (k < nb) {
        float y[8];
#pragma unroll
        for (int e = 0; e < 4; ++e) {
          y[2 * e] = fmaf(o0, bf_lo(wor[k][0][e]), o1 * bf_lo(wor[k][1][e]));
          y[2 * e + 1] = fmaf(o0, bf_hi(wor[k][0][e]), o1 * bf_hi(wor[k][1][e]));
        }
#pragma unroll
        for (int e = 0; e < 8; ++e) { y[e] += dpp<XOR8>(y[e]); y[e] = xrow_sum(y[e]); }
        if (r8 == 0) {
          *reinterpret_cast<LAS f32x4*>(yp + ci * 320 + 64 * k + 8 * c8) = f32x4{y[0], y[1], y[2], y[3]};
          *reinterpret_cast<LAS f32x4*>(yp + ci * 320 + 64 * k + 8 * c8 + 4) = f32x4{y[4], y[5], y[6], y[7]};
        }
      }
    }
  }
  BAR();
  const u32x2 tag = {ctl[12], ctl[13]};
  if (ctid < 32 * nb) {
    float s0 = 0.f, s1 = 0.f;
#pragma unroll
    for (int u = 0; u < NCONS; ++u) { s0 += yp[u * 320 + 2 * ctid]; s1 += yp[u * 320 + 2 * ctid + 1]; }
    st_g16(ysl + ctid, s0, s1, tag);
  }
  if (ci == NCONS - 1 && b < 160) {
    const int br = b / 80, col0 = 8 * (b % 80);
    const u32x4* g = ws + Y0 + (size_t)(NH * br + (lane >> 2)) * (DM / 2) + col0 / 2 + (lane & 3);
    u32x4 v0, v1;
    for (unsigned sp = 0; sp < SPIN_CAP; ++sp) {
      ld_g16x2(g, g + (size_t)16 * (DM / 2), v0, v1);
      if (__all(gok(v0, tag) && gok(v1, tag))) break;
      __builtin_amdgcn_s_sleep(1);
    }
    float s0 = __uint_as_float(v0[0]) + __uint_as_float(v1[0]), s1 = __uint_as_float(v0[2]) + __uint_as_float(v1[2]);
    s0 += dpp<0x124>(s0); s1 += dpp<0x124>(s1);
    s0 += dpp<XOR8>(s0); s1 += dpp<XOR8>(s1);
    s0 = xrow_sum(s0); s1 = xrow_sum(s1);
    if (lane < 4) reinterpret_cast<unsigned*>(out + br * DM + col0)[lane] = pack_bf16(s0, s1);
  }
  STAMP(7);
}

extern "C" void kernel_launch(void* const* d_in, const int* in_sizes, int n_in,
                              void* d_out, int out_size, void* d_ws, size_t ws_size,
                              hipStream_t stream) {
  typedef const __hip_bfloat16* P;
#define XA
  attn_main<<<NWG, NTHR, 0, stream>>>((P)d_in[0], (P)d_in[1], (P)d_in[2], (P)d_in[3], (P)d_in[4],
                                      (P)d_in[5], (P)d_in[6], (P)d_in[7], (P)d_in[8], (P)d_in[9],
                                      (u32x4*)d_ws, (__hip_bfloat16*)d_out XA);
}
```

```cpp
#include <hip/hip_runtime.h>
#include <hip/hip_bf16.h>
#include <stdint.h>
#include <type_traits>

typedef unsigned u32x4 __attribute__((ext_vector_type(4)));
typedef unsigned u32x2 __attribute__((ext_vector_type(2)));
typedef float f32x4 __attribute__((ext_vector_type(4)));
typedef __bf16 bf16x2 __attribute__((ext_vector_type(2)));
#define LAS __attribute__((address_space(3)))

constexpr int DM = 640, NH = 32, DH = 128, SK = 8192, WIN = 2048;
typedef unsigned long long u64;
constexpr int NCONS = 8, NLOAD = 2, NTHR = (NCONS + NLOAD + 1) * 64;
constexpr int SPLIT_A = 6, SPLIT_B = 2;
constexpr int NWG_A = NH * SPLIT_A, NWG_B = NH * SPLIT_B, NWG = NWG_A + NWG_B;
constexpr float SC2 = 0.088388347648318440f * 1.4426950408889634f;
#define RR 18
#define IR 5
constexpr int ROWB = 8192;
constexpr unsigned SPIN_CAP = 1u << 20;
constexpr int QP_OFF = RR * ROWB, AP_OFF = QP_OFF + 4096, ML_OFF = AP_OFF + 4096, QF_OFF = ML_OFF + 64, CT_OFF = QF_OFF + 256, LDS_BYTES = CT_OFF + 64;
static_assert(LDS_BYTES <= 163840 && RR * ROWB >= NCONS * 320 * 4, "LDS");
constexpr int FLAG0 = 64, GR0 = 512, GSTR = 136, Y0 = GR0 + GSTR * NWG;

#define SB() do { asm volatile("" ::: "memory"); __builtin_amdgcn_sched_barrier(0); } while (0)
#define PIN(x) asm volatile("" : "+v"(x))
#define LGKM0() asm volatile("s_waitcnt lgkmcnt(0)" ::: "memory")
#define BAR() asm volatile("s_waitcnt lgkmcnt(0)\n\ts_barrier" ::: "memory")

__device__ __forceinline__ float dot2(unsigned k, unsigned q, float acc) {
  return __builtin_amdgcn_fdot2_f32_bf16(__builtin_bit_cast(bf16x2, k), __builtin_bit_cast(bf16x2, q), acc, false);
}
template <int CTRL> __device__ __forceinline__ float dpp(float x) {
  return __builtin_bit_cast(float, __builtin_amdgcn_mov_dpp(__builtin_bit_cast(int, x), CTRL, 0xf, 0xf, true));
}
constexpr int XOR1 = 0xB1, XOR2 = 0x4E, XOR7 = 0x141, XOR8 = 0x128;
template <int T> __device__ __forceinline__ float bcast16(float p) { return dpp<0x150 + T>(p); }
__device__ __forceinline__ float xrow_max(float x) {
  auto s = __builtin_amdgcn_permlane16_swap(__float_as_uint(x), __float_as_uint(x), false, false);
  x = fmaxf(__uint_as_float(s[0]), __uint_as_float(s[1]));
  auto t = __builtin_amdgcn_permlane32_swap(__float_as_uint(x), __float_as_uint(x), false, false);
  return fmaxf(__uint_as_float(t[0]), __uint_as_float(t[1]));
}
__device__ __forceinline__ float xrow_sum(float x) {
  auto s = __builtin_amdgcn_permlane16_swap(__float_as_uint(x), __float_as_uint(x), false, false);
  x = __uint_as_float(s[0]) + __uint_as_float(s[1]);
  auto t = __builtin_amdgcn_permlane32_swap(__float_as_uint(x), __float_as_uint(x), false, false);
  return __uint_as_float(t[0]) + __uint_as_float(t[1]);
}
__device__ __forceinline__ float wave_max(float x) {
  x = fmaxf(x, dpp<XOR1>(x)); x = fmaxf(x, dpp<XOR2>(x)); x = fmaxf(x, dpp<XOR7>(x)); x = fmaxf(x, dpp<XOR8>(x));
  return xrow_max(x);
}
__device__ __forceinline__ float wave_sum(float x) {
  x += dpp<XOR1>(x); x += dpp<XOR2>(x); x += dpp<XOR7>(x); x += dpp<XOR8>(x);
  return xrow_sum(x);
}
__device__ __forceinline__ float bf_lo(unsigned u) { return __uint_as_float(u << 16); }
__device__ __forceinline__ float bf_hi(unsigned u) { return __uint_as_float(u & 0xffff0000u); }
__device__ __forceinline__ unsigned pack_bf16(float lo, float hi) {
  unsigned r; asm("v_cvt_pk_bf16_f32 %0, %1, %2" : "=v"(r) : "v"(lo), "v"(hi)); return r;
}
__device__ __forceinline__ u64 ld_g(const u64* p) { return __hip_atomic_load(p, __ATOMIC_RELAXED, __HIP_MEMORY_SCOPE_AGENT); }
__device__ __forceinline__ void st_g(u64* p, u64 v) { __hip_atomic_store(p, v, __ATOMIC_RELAXED, __HIP_MEMORY_SCOPE_AGENT); }
__device__ __forceinline__ void st_g16(u32x4* p, float v0, float v1, u32x2 tag) {
  const u32x4 v = {__float_as_uint(v0), tag[0], __float_as_uint(v1), tag[1]};
  asm volatile("global_store_dwordx4 %0, %1, off sc1\n\ts_nop 1" :: "v"(p), "v"(v) : "memory");
}
__device__ __forceinline__ void ld_g16x2(const u32x4* p0, const u32x4* p1, u32x4& a, u32x4& b) {
  asm volatile("global_load_dwordx4 %0, %2, off sc1\n\tglobal_load_dwordx4 %1, %3, off sc1\n\ts_waitcnt vmcnt(0)"
               : "=&v"(a), "=&v"(b) : "v"(p0), "v"(p1) : "memory");
}
__device__ __forceinline__ void ld_g16x4(const u32x4* p0, const u32x4* p1, const u32x4* p2, const u32x4* p3, u32x4& a, u32x4& b, u32x4& c, u32x4& d) {
  asm volatile("global_load_dwordx4 %0, %4, off sc1\n\tglobal_load_dwordx4 %1, %5, off sc1\n\t"
               "global_load_dwordx4 %2, %6, off sc1\n\tglobal_load_dwordx4 %3, %7, off sc1\n\ts_waitcnt vmcnt(0)"
               : "=&v"(a), "=&v"(b), "=&v"(c), "=&v"(d) : "v"(p0), "v"(p1), "v"(p2), "v"(p3) : "memory");
}
__device__ __forceinline__ void ld_g16x6(const u32x4* p, const int (&o)[6], u32x4 (&v)[6]) {
  asm volatile("global_load_dwordx4 %0, %6, off sc1\n\tglobal_load_dwordx4 %1, %7, off sc1\n\t"
               "global_load_dwordx4 %2, %8, off sc1\n\tglobal_load_dwordx4 %3, %9, off sc1\n\t"
               "global_load_dwordx4 %4, %10, off sc1\n\tglobal_load_dwordx4 %5, %11, off sc1\n\ts_waitcnt vmcnt(0)"
               : "=&v"(v[0]), "=&v"(v[1]), "=&v"(v[2]), "=&v"(v[3]), "=&v"(v[4]), "=&v"(v[5])
               : "v"(p + o[0]), "v"(p + o[1]), "v"(p + o[2]), "v"(p + o[3]), "v"(p + o[4]), "v"(p + o[5]) : "memory");
}
__device__ __forceinline__ bool gok(const u32x4 g, u32x2 tag) { return g[1] == tag[0] && g[3] == tag[1]; }
__device__ __forceinline__ u32x4 ldw(const u32x4* p) { return __builtin_nontemporal_load(p); }

__device__ __forceinline__ float transpose_reduce16(float (&x)[16], int c) {
#define TR_STEP(HALF, CTRL, BIT)                                                          \
  _Pragma("unroll") for (int h = 0; h < (HALF); ++h) {                                    \
    const float lo_ = x[h], hi_ = x[h + (HALF)];                                          \
    const float t1_ = lo_ + dpp<CTRL>(lo_), t2_ = hi_ + dpp<CTRL>(hi_);                   \
    x[h] = (c & (BIT)) ? t2_ : t1_; }
  TR_STEP(8, XOR8, 8) TR_STEP(4, XOR7, 4) TR_STEP(2, XOR2, 2) TR_STEP(1, XOR1, 1)
#undef TR_STEP
  return x[0];
}
template <int T>
__device__ __forceinline__ void pv_step(const u32x4 d, float p, float (&acc)[8]) {
  const float pt = bcast16<T>(p);
#pragma unroll
  for (int k = 0; k < 4; ++k) {
    acc[2 * k] = fmaf(pt, bf_lo(d[k]), acc[2 * k]);
    acc[2 * k + 1] = fmaf(pt, bf_hi(d[k]), acc[2 * k + 1]);
  }
}

__device__ __forceinline__ void dma4k(const void* gsrc, unsigned lds_dst) {
  unsigned keep;
  asm volatile("s_mov_b32 %0, m0\n\ts_mov_b32 m0, %2\n\ts_nop 0\n\t"
               "global_load_lds_dwordx4 %1, off nt\n\t"
               "global_load_lds_dwordx4 %1, off offset:1024 nt\n\t"
               "global_load_lds_dwordx4 %1, off offset:2048 nt\n\t"
               "global_load_lds_dwordx4 %1, off offset:3072 nt\n\t"
               "s_mov_b32 m0, %0" : "=&s"(keep) : "v"(gsrc), "s"(lds_dst) : "memory");
}
template <int N> __device__ __forceinline__ void vmw() { asm volatile("s_waitcnt vmcnt(%0)" :: "n"(N) : "memory"); }
template <int K> __device__ __forceinline__ void drain_ladder(volatile LAS unsigned* full, int total, int pub) {
  if constexpr (K >= 0) { vmw<4 * K>(); if (total - K > pub) *full = total - K; drain_ladder<K - 1>(full, total, pub); }
}
__device__ __forceinline__ int wrapr(int s) { return s >= RR ? s - RR : s; }

__global__ void __launch_bounds__(NTHR)
attn_main(const __hip_bfloat16* __restrict__ ax, const __hip_bfloat16* __restrict__ ak, const __hip_bfloat16* __restrict__ av,
          const __hip_bfloat16* __restrict__ awq, const __hip_bfloat16* __restrict__ awo,
          const __hip_bfloat16* __restrict__ bx, const __hip_bfloat16* __restrict__ bk, const __hip_bfloat16* __restrict__ bv,
          const __hip_bfloat16* __restrict__ bwq, const __hip_bfloat16* __restrict__ bwo,
          u32x4* ws, __hip_bfloat16* out
          ) {
  __shared__ __attribute__((aligned(16))) unsigned char lds[LDS_BYTES];
  LAS unsigned char* L8 = (LAS unsigned char*)lds;
  volatile LAS unsigned* ctl = (volatile LAS unsigned*)(L8 + CT_OFF);
  const int lane = threadIdx.x & 63, w = __builtin_amdgcn_readfirstlane(threadIdx.x >> 6);
  const int b = blockIdx.x, xcd = b & 7, bi = b >> 3;
  const bool isA = bi < 24;
  int h, j, T, team0, row0, n, sb, nb;
  if (isA) {
    h = 4 * xcd + bi / SPLIT_A; j = bi % SPLIT_A; T = SPLIT_A; team0 = h * SPLIT_A;
    row0 = (256 * j) / SPLIT_A; n = (256 * (j + 1)) / SPLIT_A - row0;
    sb = j < 4 ? 2 * j : j + 4; nb = j < 4 ? 2 : 1;
  } else {
    h = 4 * xcd + (bi - 24) / SPLIT_B; j = (bi - 24) % SPLIT_B; T = SPLIT_B; team0 = NWG_A + h * SPLIT_B;
    row0 = (SK - WIN) / 32 + 32 * j; n = 32;
    sb = 5 * j; nb = 5;
  }
  const int wg = team0 + j;
  u32x4* ysl = ws + Y0 + (size_t)((isA ? 0 : NH) + h) * (DM / 2) + 32 * sb;
  if (w < NLOAD) {
    const size_t off = ((size_t)h * (SK / 32) + row0) * ROWB + w * 4096 + lane * 16;
    const char* kp = (const char*)(isA ? ak : bk) + off;
    const char* vp = (const char*)(isA ? av : bv) + off - (size_t)n * ROWB;
    volatile LAS unsigned* full = ctl + 8 + w;
    const unsigned lbase = (unsigned)(size_t)L8 + w * 4096;
    asm volatile("s_barrier" ::: "memory");
#pragma unroll
    for (int R = 0; R < IR; ++R) dma4k(kp + (size_t)R * ROWB, lbase + R * ROWB);
    int slot = IR, lim = RR, pub = 0;
#pragma unroll 1
    for (int R = IR; R < 2 * n; ++R) {
      if (R >= lim) {
        bool drained = false;
        for (unsigned sp = 0; sp < SPIN_CAP; ++sp) {
          const u32x4 f = *(volatile LAS u32x4*)(ctl + 4 * w);
          lim = __builtin_amdgcn_readfirstlane(min(min(f[0], f[1]), min(f[2], f[3]))) + RR;
          if (R < lim) break;
          if (!drained) { vmw<0>(); *full = R; pub = R; drained = true; }
          __builtin_amdgcn_s_sleep(1);
        }
        asm volatile("" ::: "memory");
      }
      dma4k((R < n ? kp : vp) + (size_t)R * ROWB, lbase + slot * ROWB);
      slot = wrapr(slot + 1);
      vmw<4 * IR>();
      if (R - IR + 1 > pub) { pub = R - IR + 1; *full = pub; }
    }
    drain_ladder<IR - 1>(full, 2 * n, pub);
    return;
  }
  LAS float* qp = (LAS float*)(L8 + QP_OFF);
  if (w == NLOAD) {
    const u64 ep = ld_g(reinterpret_cast<const u64*>(ws));
    if (lane < 16) ctl[lane] = 0u;
    BAR();
    const u64 tag64 = ep + 1ull;
    const u32x2 tag = {(unsigned)tag64, (unsigned)(tag64 >> 32)};
    if (lane == 0) st_g(reinterpret_cast<u64*>(ws + FLAG0) + wg, tag64);
    if (lane < 2) ctl[12 + lane] = tag[lane];
    for (unsigned sp = 0; ctl[10] < (unsigned)NCONS && sp < SPIN_CAP; ++sp) __builtin_amdgcn_s_sleep(1);
    SB();
    {
      float s0 = 0.f, s1 = 0.f;
#pragma unroll
      for (int u = 0; u < NCONS; ++u) { s0 += qp[u * DH + 2 * lane]; s1 += qp[u * DH + 2 * lane + 1]; }
      st_g16(ws + GR0 + (size_t)GSTR * wg + lane, s0, s1, tag);
    }
    {
      const u32x4* g = ws + GR0 + (size_t)GSTR * team0 + lane;
      int o[6];
#pragma unroll
      for (int u = 0; u < SPLIT_A; ++u) o[u] = GSTR * min(u, T - 1);
      u32x4 v[6];
      for (unsigned sp = 0; sp < SPIN_CAP; ++sp) {
        ld_g16x6(g, o, v);
        bool ok = true;
#pragma unroll
        for (int u = 0; u < SPLIT_A; ++u) ok &= gok(v[u], tag);
        if (__all(ok)) break;
        __builtin_amdgcn_s_sleep(1);
      }
      float s0 = 0.f, s1 = 0.f;
#pragma unroll
      for (int u = 0; u < SPLIT_A; ++u) if (u < T) { s0 += __uint_as_float(v[u][0]); s1 += __uint_as_float(v[u][2]); }
      ((LAS unsigned*)(L8 + QF_OFF))[lane] = pack_bf16(s0, s1);
      LGKM0();
      if (lane == 0) ctl[11] = 1u;
    }
    if (b == 0) {
      for (unsigned sp = 0; sp < SPIN_CAP; ++sp) {
        bool ok = true;
#pragma unroll
        for (int k = 0; k < 4; ++k) ok &= ld_g(reinterpret_cast<const u64*>(ws + FLAG0) + 64 * k + lane) == tag64;
        if (__all(ok)) break;
        __builtin_amdgcn_s_sleep(8);
      }
      if (lane == 0) st_g(reinterpret_cast<u64*>(ws), tag64);
    }
    return;
  }
  const int ci = w - NLOAD - 1, ctid = ci * 64 + lane;
  const int r = lane >> 4, c = lane & 15, r8 = lane >> 3, c8 = lane & 7;
  LAS float* ap = (LAS float*)(L8 + AP_OFF);
  LAS float* ml = (LAS float*)(L8 + ML_OFF);
  LAS float* yp = (LAS float*)L8;
  u32x4 wor[5][2];
  {
    const unsigned short* x = reinterpret_cast<const unsigned short*>(isA ? ax : bx);
    const __hip_bfloat16* wq = (isA ? awq : bwq) + h * DH + 8 * c;
    const int qr0 = (DM * j) / T, qcnt = (DM * (j + 1)) / T - qr0;
    auto prologue = [&](auto nq_, auto nb_) {
      constexpr int NQ = decltype(nq_)::value, NB = decltype(nb_)::value;
      u32x4 wr[NQ]; unsigned xs[NQ];
      asm volatile("; prologue %0" :: "n"(NQ) : "memory");
#pragma unroll
      for (int i = 0; i < NQ; ++i) {
        const int row = qr0 + min(32 * i + 4 * ci + r, qcnt - 1);
        wr[i] = ldw(reinterpret_cast<const u32x4*>(wq + (size_t)row * (NH * DH)));
        xs[i] = x[row];
      }
      SB();
      BAR();
      SB();
      float qa[8];
#pragma unroll
      for (int k = 0; k < 8; ++k) qa[k] = 0.f;
#pragma unroll
      for (int i = 0; i < NQ; ++i) {
        asm volatile("" : "+v"(wr[i]), "+v"(xs[i]));
        const float xv = (32 * i + 4 * ci + r < qcnt) ? __uint_as_float(xs[i] << 16) : 0.f;
#pragma unroll
        for (int k = 0; k < 4; ++k) {
          qa[2 * k] = fmaf(xv, bf_lo(wr[i][k]), qa[2 * k]);
          qa[2 * k + 1] = fmaf(xv, bf_hi(wr[i][k]), qa[2 * k + 1]);
        }
      }
#pragma unroll
      for (int k = 0; k < 8; ++k) qa[k] = xrow_sum(qa[k]);
      if (r == 0) {
        *reinterpret_cast<LAS f32x4*>(qp + ci * DH + 8 * c) = f32x4{qa[0], qa[1], qa[2], qa[3]};
        *reinterpret_cast<LAS f32x4*>(qp + ci * DH + 8 * c + 4) = f32x4{qa[4], qa[5], qa[6], qa[7]};
      }
      __builtin_amdgcn_s_waitcnt(0);
      SB();
      if (lane == 0) __hip_atomic_fetch_add((LAS unsigned*)(ctl + 10), 1u, __ATOMIC_RELAXED, __HIP_MEMORY_SCOPE_WORKGROUP);
      SB();
    };
    if (isA) prologue(std::integral_constant<int, 4>{}, std::integral_constant<int, 2>{});
    else prologue(std::integral_constant<int, 10>{}, std::integral_constant<int, 5>{});
    SB();
  }
  const int G = (n + 15) >> 4;
  volatile LAS unsigned* full = ctl + 8 + (ci >> 2);
  LAS unsigned char* mine = L8 + ci * 1024 + lane * 16;
  int F = 0, s0r = 0;
#define FETCH4(R0, base, d)                                                                     \
    const int cnt_ = min(4, n - (R0)), need_ = (base) + (R0) + cnt_;                            \
    for (unsigned sp = 0; F < need_ && sp < SPIN_CAP; ++sp) { F = __builtin_amdgcn_readfirstlane(*full); if (F < need_) __builtin_amdgcn_s_sleep(1); } \
    asm volatile("" ::: "memory");                                                              \
    _Pragma("unroll") for (int t = 0; t < 4; ++t)                                               \
      (d)[t] = *reinterpret_cast<const LAS u32x4*>(mine + wrapr(s0r + min(t, cnt_ - 1)) * ROWB);  \
    LGKM0();                                                                                    \
    if (lane == 0) ctl[ci] = need_;                                                             \
    asm volatile("" : "+v"((d)[0]), "+v"((d)[1]), "+v"((d)[2]), "+v"((d)[3]) :: "memory");      \
    s0r = wrapr(s0r + cnt_);
  u32x4 q = {0u, 0u, 0u, 0u};
  float sc0 = -INFINITY, sc1 = -INFINITY, sc2 = -INFINITY;
#pragma unroll 1
  for (int g = 0; g < G; ++g) {
    u32x4 kd[16];
#pragma unroll
    for (int bb = 0; bb < 4; ++bb) {
      const int R0 = 16 * g + 4 * bb;
      if (R0 < n) { FETCH4(R0, 0, kd + 4 * bb) }
      else {
#pragma unroll
        for (int t = 0; t < 4; ++t) kd[4 * bb + t] = u32x4{0u, 0u, 0u, 0u};
      }
    }
    if (g == 0) {
      for (unsigned sp = 0; ctl[11] == 0u && sp < SPIN_CAP; ++sp) __builtin_amdgcn_s_sleep(1);
      SB();
      q = *reinterpret_cast<const LAS u32x4*>(L8 + QF_OFF + 16 * c);
    }
    float xx[16];
#pragma unroll
    for (int t = 0; t < 16; ++t) {
      float a = 0.f;
#pragma unroll
      for (int k = 0; k < 4; ++k) a = dot2(kd[t][k], q[k], a);
      xx[t] = a;
    }
    float v = transpose_reduce16(xx, c) * SC2;
    v = (16 * g + c < n) ? v : -INFINITY;
    sc2 = sc1; sc1 = sc0; sc0 = v;
  }
  const float m = wave_max(fmaxf(fmaxf(sc0, sc1), sc2));
  const float p0 = __builtin_amdgcn_exp2f(sc0 - m), p1 = __builtin_amdgcn_exp2f(sc1 - m), p2 = __builtin_amdgcn_exp2f(sc2 - m);
  const float l = wave_sum(p0 + p1 + p2);
  const __hip_bfloat16* wop = (isA ? awo : bwo) + (size_t)(h * DH + 16 * ci + r8) * DM + 64 * sb + 8 * c8;
#pragma unroll
  for (int k = 0; k < 5; ++k) asm volatile("" : "=v"(wor[k][0]), "=v"(wor[k][1]));
#define WOLOAD(NB) do { _Pragma("unroll") for (int k = 0; k < (NB); ++k) { const int kk = min(k, nb - 1);     \
      wor[k][0] = ldw(reinterpret_cast<const u32x4*>(wop + 64 * kk));                                          \
      wor[k][1] = ldw(reinterpret_cast<const u32x4*>(wop + (size_t)8 * DM + 64 * kk)); } SB(); } while (0)
  float acc[8];
#pragma unroll
  for (int k = 0; k < 8; ++k) acc[k] = 0.f;
#pragma unroll 1
  for (int g = 0; g < G; ++g) {
    const int back = G - 1 - g;
    const float p = back == 0 ? p0 : back == 1 ? p1 : p2;
#define PVB(BB) { const int R0 = 16 * g + 4 * (BB); if (R0 < n) { u32x4 d[4]; FETCH4(R0, n, d)  \
      pv_step<4 * (BB)>(d[0], p, acc); pv_step<4 * (BB) + 1>(d[1], p, acc); pv_step<4 * (BB) + 2>(d[2], p, acc); pv_step<4 * (BB) + 3>(d[3], p, acc); } }
    PVB(0) PVB(1)
    if (isA && g == 2) WOLOAD(2);
    PVB(2)
    if (!isA && g == 1) WOLOAD(5);
    PVB(3)
#undef PVB
  }
#pragma unroll
  for (int k = 0; k < 8; ++k) acc[k] = xrow_sum(acc[k]);
  if (r == 0) {
    *reinterpret_cast<LAS f32x4*>(ap + ci * DH + 8 * c) = f32x4{acc[0], acc[1], acc[2], acc[3]};
    *reinterpret_cast<LAS f32x4*>(ap + ci * DH + 8 * c + 4) = f32x4{acc[4], acc[5], acc[6], acc[7]};
    if (c == 0) { ml[2 * ci] = m; ml[2 * ci + 1] = l; }
  }
  BAR();
  float of;
  {
    float M = -INFINITY, L = 0.f, o = 0.f;
#pragma unroll
    for (int u = 0; u < NCONS; ++u) M = fmaxf(M, ml[2 * u]);
#pragma unroll
    for (int u = 0; u < NCONS; ++u) {
      const float sc = __builtin_amdgcn_exp2f(ml[2 * u] - M);
      L = fmaf(sc, ml[2 * u + 1], L);
      o = fmaf(sc, ap[u * DH + 16 * ci + c], o);
    }
    for (unsigned sp = 0; ctl[11] == 0u && sp < SPIN_CAP; ++sp) __builtin_amdgcn_s_sleep(1);
    const u32x2 tag = {ctl[12], ctl[13]};
    u32x4* gr = ws + GR0 + (size_t)GSTR * wg + 64;
    const float on = dpp<XOR1>(o);
    if (lane < 16 && !(lane & 1)) st_g16(gr + 8 * ci + (lane >> 1), o, on, tag);
    else if (ci == 0 && lane == 16) st_g16(gr + 64, M, L, tag);
    const bool a0 = r < T, a1 = r + 4 < T;
    const u32x4* g0 = ws + GR0 + (size_t)GSTR * (team0 + (a0 ? r : 0)) + 64;
    const u32x4* g1 = ws + GR0 + (size_t)GSTR * (team0 + (a1 ? r + 4 : 0)) + 64;
    u32x4 vo0, vm0, vo1, vm1;
    for (unsigned sp = 0; sp < SPIN_CAP; ++sp) {
      ld_g16x4(g0 + 8 * ci + (c >> 1), g0 + 64, g1 + 8 * ci + (c >> 1), g1 + 64, vo0, vm0, vo1, vm1);
      if (__all(gok(vo0, tag) && gok(vm0, tag) && gok(vo1, tag) && gok(vm1, tag))) break;
      __builtin_amdgcn_s_sleep(1);
    }
    const float m0 = a0 ? __uint_as_float(vm0[0]) : -INFINITY, m1 = a1 ? __uint_as_float(vm1[0]) : -INFINITY;
    const float Mh = xrow_max(fmaxf(m0, m1));
    const float e0 = __builtin_amdgcn_exp2f(m0 - Mh), e1 = __builtin_amdgcn_exp2f(m1 - Mh);
    const float Lh = xrow_sum(fmaf(e0, __uint_as_float(vm0[2]), e1 * __uint_as_float(vm1[2])));
    const float x0 = __uint_as_float((c & 1) ? vo0[2] : vo0[0]), x1 = __uint_as_float((c & 1) ? vo1[2] : vo1[0]);
    of = xrow_sum(fmaf(e0, x0, e1 * x1)) / Lh;
  }
  {
    const float o0 = __shfl(of, r8), o1 = __shfl(of, 8 + r8);
#pragma unroll
    for (int k = 0; k < 5; ++k) {
      if (k < nb) {
        float y[8];
#pragma unroll
        for (int e = 0; e < 4; ++e) {
          y[2 * e] = fmaf(o0, bf_lo(wor[k][0][e]), o1 * bf_lo(wor[k][1][e]));
          y[2 * e + 1] = fmaf(o0, bf_hi(wor[k][0][e]), o1 * bf_hi(wor[k][1][e]));
        }
#pragma unroll
        for (int e = 0; e < 8; ++e) { y[e] += dpp<XOR8>(y[e]); y[e] = xrow_sum(y[e]); }
        if (r8 == 0) {
          *reinterpret_cast<LAS f32x4*>(yp + ci * 320 + 64 * k + 8 * c8) = f32x4{y[0], y[1], y[2], y[3]};
          *reinterpret_cast<LAS f32x4*>(yp + ci * 320 + 64 * k + 8 * c8 + 4) = f32x4{y[4], y[5], y[6], y[7]};
        }
      }
    }
  }
  BAR();
  const u32x2 tag = {ctl[12], ctl[13]};
  if (ctid < 32 * nb) {
    float s0 = 0.f, s1 = 0.f;
#pragma unroll
    for (int u = 0; u < NCONS; ++u) { s0 += yp[u * 320 + 2 * ctid]; s1 += yp[u * 320 + 2 * ctid + 1]; }
    st_g16(ysl + ctid, s0, s1, tag);
  }
  if (ci == NCONS - 1 && b < 160) {
    const int br = b / 80, col0 = 8 * (b % 80);
    const u32x4* g = ws + Y0 + (size_t)(NH * br + (lane >> 2)) * (DM / 2) + col0 / 2 + (lane & 3);
    u32x4 v0, v1;
    for (unsigned sp = 0; sp < SPIN_CAP; ++sp) {
      ld_g16x2(g, g + (size_t)16 * (DM / 2), v0, v1);
      if (__all(gok(v0, tag) && gok(v1, tag))) break;
      __builtin_amdgcn_s_sleep(1);
    }
    float s0 = __uint_as_float(v0[0]) + __uint_as_float(v1[0]), s1 = __uint_as_float(v0[2]) + __uint_as_float(v1[2]);
    s0 += dpp<0x124>(s0); s1 += dpp<0x124>(s1);
    s0 += dpp<XOR8>(s0); s1 += dpp<XOR8>(s1);
    s0 = xrow_sum(s0); s1 = xrow_sum(s1);
    if (lane < 4) reinterpret_cast<unsigned*>(out + br * DM + col0)[lane] = pack_bf16(s0, s1);
  }
}

extern "C" void kernel_launch(void* const* d_in, const int* in_sizes, int n_in,
                              void* d_out, int out_size, void* d_ws, size_t ws_size,
                              hipStream_t stream) {
  typedef const __hip_bfloat16* P;
  attn_main<<<NWG, NTHR, 0, stream>>>((P)d_in[0], (P)d_in[1], (P)d_in[2], (P)d_in[3], (P)d_in[4],
                                      (P)d_in[5], (P)d_in[6], (P)d_in[7], (P)d_in[8], (P)d_in[9],
                                      (u32x4*)d_ws, (__hip_bfloat16*)d_out);
}
```

```cpp
#define MAPV 1
#include <hip/hip_runtime.h>
#include <hip/hip_bf16.h>
#include <stdint.h>
#include <type_traits>

typedef unsigned u32x4 __attribute__((ext_vector_type(4)));
typedef unsigned u32x2 __attribute__((ext_vector_type(2)));
typedef float f32x4 __attribute__((ext_vector_type(4)));
typedef float f32x2 __attribute__((ext_vector_type(2)));
typedef __bf16 bf16x2 __attribute__((ext_vector_type(2)));
typedef __bf16 bf16x8 __attribute__((ext_vector_type(8)));
#define LAS __attribute__((address_space(3)))

constexpr int DM = 640, NH = 32, DH = 128, SK = 8192, WIN = 2048;
typedef unsigned long long u64;
constexpr int NCONS = 8, NLOAD = 2, NTHR = (NCONS + NLOAD + 1) * 64;
constexpr int SPLIT_A = 6, SPLIT_B = 2;
constexpr int NWG_A = NH * SPLIT_A, NWG_B = NH * SPLIT_B, NWG = NWG_A + NWG_B;
constexpr float SC2 = 0.088388347648318440f * 1.4426950408889634f;
#define RR 18
#define IR 5
constexpr int ROWB = 8192;
constexpr unsigned SPIN_CAP = 1u << 20;
#define WLATE 0
#define WPA 10
#define WPB 7
constexpr int APS = 132, YPS = 384;
constexpr int QP_OFF = RR * ROWB, AP_OFF = QP_OFF + 4096, ML_OFF = AP_OFF + NCONS * APS * 4, QF_OFF = ML_OFF + 64, CT_OFF = QF_OFF + 256, LDS_BYTES = CT_OFF + 128;
static_assert(LDS_BYTES <= 163840 && RR * ROWB >= NCONS * YPS * 4 && ML_OFF % 16 == 0, "LDS");
constexpr int FLAG0 = 64, GR0 = 512, GSTR = 136, Y0 = GR0 + GSTR * NWG;
#define STAMP(i)

#define SB() do { asm volatile("" ::: "memory"); __builtin_amdgcn_sched_barrier(0); } while (0)
#define PIN(x) asm volatile("" : "+v"(x))
#define LGKM0() asm volatile("s_waitcnt lgkmcnt(0)" ::: "memory")
#define BAR() asm volatile("s_waitcnt lgkmcnt(0)\n\ts_barrier" ::: "memory")

__device__ __forceinline__ float dot2(unsigned k, unsigned q, float acc) {
  return __builtin_amdgcn_fdot2_f32_bf16(__builtin_bit_cast(bf16x2, k), __builtin_bit_cast(bf16x2, q), acc, false);
}
template <int CTRL> __device__ __forceinline__ float dpp(float x) {
  return __builtin_bit_cast(float, __builtin_amdgcn_mov_dpp(__builtin_bit_cast(int, x), CTRL, 0xf, 0xf, true));
}
constexpr int XOR1 = 0xB1, XOR2 = 0x4E, XOR7 = 0x141, XOR8 = 0x128;
template <int T> __device__ __forceinline__ float bcast16(float p) { return dpp<0x150 + T>(p); }
__device__ __forceinline__ float xrow_max(float x) {
  auto s = __builtin_amdgcn_permlane16_swap(__float_as_uint(x), __float_as_uint(x), false, false);
  x = fmaxf(__uint_as_float(s[0]), __uint_as_float(s[1]));
  auto t = __builtin_amdgcn_permlane32_swap(__float_as_uint(x), __float_as_uint(x), false, false);
  return fmaxf(__uint_as_float(t[0]), __uint_as_float(t[1]));
}
__device__ __forceinline__ float xrow_sum(float x) {
  auto s = __builtin_amdgcn_permlane16_swap(__float_as_uint(x), __float_as_uint(x), false, false);
  x = __uint_as_float(s[0]) + __uint_as_float(s[1]);
  auto t = __builtin_amdgcn_permlane32_swap(__float_as_uint(x), __float_as_uint(x), false, false);
  return __uint_as_float(t[0]) + __uint_as_float(t[1]);
}
__device__ __forceinline__ float wave_max(float x) {
  x = fmaxf(x, dpp<XOR1>(x)); x = fmaxf(x, dpp<XOR2>(x)); x = fmaxf(x, dpp<XOR7>(x)); x = fmaxf(x, dpp<XOR8>(x));
  return xrow_max(x);
}
__device__ __forceinline__ float wave_sum(float x) {
  x += dpp<XOR1>(x); x += dpp<XOR2>(x); x += dpp<XOR7>(x); x += dpp<XOR8>(x);
  return xrow_sum(x);
}
__device__ __forceinline__ float bf_lo(unsigned u) { return __uint_as_float(u << 16); }
__device__ __forceinline__ float bf_hi(unsigned u) { return __uint_as_float(u & 0xffff0000u); }
__device__ __forceinline__ unsigned pack_bf16(float lo, float hi) {
  unsigned r; asm("v_cvt_pk_bf16_f32 %0, %1, %2" : "=v"(r) : "v"(lo), "v"(hi)); return r;
}
__device__ __forceinline__ u64 ld_g(const u64* p) { return __hip_atomic_load(p, __ATOMIC_RELAXED, __HIP_MEMORY_SCOPE_AGENT); }
__device__ __forceinline__ void st_g(u64* p, u64 v) { __hip_atomic_store(p, v, __ATOMIC_RELAXED, __HIP_MEMORY_SCOPE_AGENT); }
__device__ __forceinline__ void st_g16(u32x4* p, float v0, float v1, u32x2 tag) {
  const u32x4 v = {__float_as_uint(v0), tag[0], __float_as_uint(v1), tag[1]};
  asm volatile("global_store_dwordx4 %0, %1, off sc1\n\ts_nop 1" :: "v"(p), "v"(v) : "memory");
}
__device__ __forceinline__ u32x4 ld_g16(const u32x4* p) {
  u32x4 a;
  asm volatile("global_load_dwordx4 %0, %1, off sc1\n\ts_waitcnt vmcnt(0)" : "=&v"(a) : "v"(p) : "memory");
  return a;
}
__device__ __forceinline__ void ld_g16x2(const u32x4* p0, const u32x4* p1, u32x4& a, u32x4& b) {
  asm volatile("global_load_dwordx4 %0, %2, off sc1\n\tglobal_load_dwordx4 %1, %3, off sc1\n\ts_waitcnt vmcnt(0)"
               : "=&v"(a), "=&v"(b) : "v"(p0), "v"(p1) : "memory");
}
__device__ __forceinline__ void ld_g16p(const u32x4* p, u32x4& a, u32x4& b) {
  asm volatile("global_load_dwordx4 %0, %2, off sc1\n\tglobal_load_dwordx4 %1, %2, off offset:16 sc1\n\ts_waitcnt vmcnt(0)"
               : "=&v"(a), "=&v"(b) : "v"(p) : "memory");
}
__device__ __forceinline__ float fold32(float x, float y) {
  auto s = __builtin_amdgcn_permlane32_swap(__float_as_uint(x), __float_as_uint(y), false, false);
  return __uint_as_float(s[0]) + __uint_as_float(s[1]);
}
__device__ __forceinline__ float fold16(float x, float y) {
  auto s = __builtin_amdgcn_permlane16_swap(__float_as_uint(x), __float_as_uint(y), false, false);
  return __uint_as_float(s[0]) + __uint_as_float(s[1]);
}
__device__ __forceinline__ void ld_g16x6(const u32x4* p, const int (&o)[6], u32x4 (&v)[6]) {
  asm volatile("global_load_dwordx4 %0, %6, off sc1\n\tglobal_load_dwordx4 %1, %7, off sc1\n\t"
               "global_load_dwordx4 %2, %8, off sc1\n\tglobal_load_dwordx4 %3, %9, off sc1\n\t"
               "global_load_dwordx4 %4, %10, off sc1\n\tglobal_load_dwordx4 %5, %11, off sc1\n\ts_waitcnt vmcnt(0)"
               : "=&v"(v[0]), "=&v"(v[1]), "=&v"(v[2]), "=&v"(v[3]), "=&v"(v[4]), "=&v"(v[5])
               : "v"(p + o[0]), "v"(p + o[1]), "v"(p + o[2]), "v"(p + o[3]), "v"(p + o[4]), "v"(p + o[5]) : "memory");
}
__device__ __forceinline__ bool gok(const u32x4 g, u32x2 tag) { return g[1] == tag[0] && g[3] == tag[1]; }
__device__ __forceinline__ u32x4 ldw(const u32x4* p) { return __builtin_nontemporal_load(p); }

__device__ __forceinline__ float transpose_reduce16(float (&x)[16], int c) {
#define TR_STEP(HALF, CTRL, BIT)                                                          \
  _Pragma("unroll") for (int h = 0; h < (HALF); ++h) {                                    \
    const float lo_ = x[h], hi_ = x[h + (HALF)];                                          \
    const float t1_ = lo_ + dpp<CTRL>(lo_), t2_ = hi_ + dpp<CTRL>(hi_);                   \
    x[h] = (c & (BIT)) ? t2_ : t1_; }
  TR_STEP(8, XOR8, 8) TR_STEP(4, XOR7, 4) TR_STEP(2, XOR2, 2) TR_STEP(1, XOR1, 1)
#undef TR_STEP
  return x[0];
}
__device__ __forceinline__ void pv_fma(const u32x4 d, float pt, float (&acc)[8]) {
#pragma unroll
  for (int k = 0; k < 4; ++k) {
    acc[2 * k] = fmaf(pt, bf_lo(d[k]), acc[2 * k]);
    acc[2 * k + 1] = fmaf(pt, bf_hi(d[k]), acc[2 * k + 1]);
  }
}

__device__ __forceinline__ void dma4k(const void* gsrc, unsigned lds_dst) {
  unsigned keep;
  asm volatile("s_mov_b32 %0, m0\n\ts_mov_b32 m0, %2\n\ts_nop 0\n\t"
               "global_load_lds_dwordx4 %1, off nt\n\t"
               "global_load_lds_dwordx4 %1, off offset:1024 nt\n\t"
               "global_load_lds_dwordx4 %1, off offset:2048 nt\n\t"
               "global_load_lds_dwordx4 %1, off offset:3072 nt\n\t"
               "s_mov_b32 m0, %0" : "=&s"(keep) : "v"(gsrc), "s"(lds_dst) : "memory");
}
template <int N> __device__ __forceinline__ void vmw() { asm volatile("s_waitcnt vmcnt(%0)" :: "n"(N) : "memory"); }
template <int K> __device__ __forceinline__ void drain_ladder(volatile LAS unsigned* full, int total, int pub) {
  if constexpr (K >= 0) { vmw<4 * K>(); if (total - K > pub) *full = total - K; drain_ladder<K - 1>(full, total, pub); }
}
__device__ __forceinline__ int wrapr(int s) { return s >= RR ? s - RR : s; }

__global__ void __launch_bounds__(NTHR)
attn_main(const __hip_bfloat16* __restrict__ ax, const __hip_bfloat16* __restrict__ ak, const __hip_bfloat16* __restrict__ av,
          const __hip_bfloat16* __restrict__ awq, const __hip_bfloat16* __restrict__ awo,
          const __hip_bfloat16* __restrict__ bx, const __hip_bfloat16* __restrict__ bk, const __hip_bfloat16* __restrict__ bv,
          const __hip_bfloat16* __restrict__ bwq, const __hip_bfloat16* __restrict__ bwo,
          u32x4* ws, __hip_bfloat16* out
          ) {
  __shared__ __attribute__((aligned(16))) unsigned char lds[LDS_BYTES];
  LAS unsigned char* L8 = (LAS unsigned char*)lds;
  volatile LAS unsigned* ctl = (volatile LAS unsigned*)(L8 + CT_OFF);
  const int lane = threadIdx.x & 63, w = __builtin_amdgcn_readfirstlane(threadIdx.x >> 6);
  const int b = blockIdx.x, xcd = b & 7, bi = b >> 3;
  const int pos = (xcd + bi) & 7; const bool isA = pos < 6; const int hh = bi, jj = isA ? pos : pos - 6;
  int h, j, T, team0, row0, n, qr0, qcnt, t0, nt;
  if (isA) {
    h = hh; j = jj; T = SPLIT_A; team0 = h * SPLIT_A;
    row0 = 42 * j + max(0, j - 3); n = 42 + (j >= 3) + (j == 5);
    qr0 = j <= 3 ? 128 * j : 192 + 64 * j; qcnt = (j == 3 || j == 4) ? 64 : 128;
    t0 = min(j, 4); nt = j < 5;
  } else {
    h = hh; j = jj; T = SPLIT_B; team0 = NWG_A + h * SPLIT_B;
    row0 = (SK - WIN) / 32 + 31 * j; n = 31 + 2 * j;
    qr0 = 320 * j; qcnt = 320;
    t0 = 3 * j; nt = 3 - j;
  }
  const int wg = team0 + j;
  if (w < NLOAD) {
    const size_t off = ((size_t)h * (SK / 32) + row0) * ROWB + w * 4096 + lane * 16;
    const char* kp = (const char*)(isA ? ak : bk) + off;
    const char* vp = (const char*)(isA ? av : bv) + off - (size_t)n * ROWB;
    volatile LAS unsigned* full = ctl + 8 + w;
    const unsigned lbase = (unsigned)(size_t)L8 + w * 4096;
    asm volatile("s_barrier" ::: "memory");
#pragma unroll
    for (int R = 0; R < IR; ++R) dma4k(kp + (size_t)R * ROWB, lbase + R * ROWB);
    int slot = IR, lim = RR, pub = 0;
#pragma unroll 1
    for (int R = IR; R < 2 * n; ++R) {
      if (R >= lim) {
        bool drained = false;
        for (unsigned sp = 0; sp < SPIN_CAP; ++sp) {
          const u32x4 f = *(volatile LAS u32x4*)(ctl + 4 * w);
          lim = __builtin_amdgcn_readfirstlane(min(min(f[0], f[1]), min(f[2], f[3]))) + RR;
          if (R < lim) break;
          if (!drained) { vmw<0>(); *full = R; pub = R; drained = true; }
          __builtin_amdgcn_s_sleep(1);
        }
        asm volatile("" ::: "memory");
      }
      dma4k((R < n ? kp : vp) + (size_t)R * ROWB, lbase + slot * ROWB);
      slot = wrapr(slot + 1);
      vmw<4 * IR>();
      if (R - IR + 1 > pub) { pub = R - IR + 1; *full = pub; }
    }
    drain_ladder<IR - 1>(full, 2 * n, pub);
    return;
  }
  LAS float* qp = (LAS float*)(L8 + QP_OFF);
  LAS float* ml = (LAS float*)(L8 + ML_OFF);
  if (w == NLOAD) {
    const u64 ep = ld_g(reinterpret_cast<const u64*>(ws));
    if (lane < 16) ctl[lane] = 0u;
    BAR();
    const u64 tag64 = ep + 1ull;
    const u32x2 tag = {(unsigned)tag64, (unsigned)(tag64 >> 32) ^ ((unsigned)tag64 * 0x9E3779B1u)};
    if (lane == 0) st_g(reinterpret_cast<u64*>(ws + FLAG0) + wg, tag64);
    if (lane < 2) ctl[12 + lane] = tag[lane];
    for (unsigned sp = 0; ctl[10] < (unsigned)NCONS && sp < SPIN_CAP; ++sp) __builtin_amdgcn_s_sleep(1);
    SB();
    {
      float s0 = 0.f, s1 = 0.f;
#pragma unroll
      for (int u = 0; u < NCONS; ++u) { s0 += qp[u * DH + 2 * lane]; s1 += qp[u * DH + 2 * lane + 1]; }
      st_g16(ws + GR0 + (size_t)GSTR * wg + lane, s0, s1, tag);
    }
    {
      const u32x4* g = ws + GR0 + (size_t)GSTR * team0 + lane;
      int o[6];
#pragma unroll
      for (int u = 0; u < SPLIT_A; ++u) o[u] = GSTR * min(u, T - 1);
      u32x4 v[6];
      for (unsigned sp = 0; sp < SPIN_CAP; ++sp) {
        ld_g16x6(g, o, v);
        bool ok = true;
#pragma unroll
        for (int u = 0; u < SPLIT_A; ++u) ok &= gok(v[u], tag);
        if (__all(ok)) break;
        __builtin_amdgcn_s_sleep(1);
      }
      float s0 = 0.f, s1 = 0.f;
#pragma unroll
      for (int u = 0; u < SPLIT_A; ++u) if (u < T) { s0 += __uint_as_float(v[u][0]); s1 += __uint_as_float(v[u][2]); }
      ((LAS unsigned*)(L8 + QF_OFF))[lane] = pack_bf16(s0, s1);
      LGKM0();
      if (lane == 0) ctl[11] = 1u;
    }
    if (b == 0) {
      for (unsigned sp = 0; sp < SPIN_CAP; ++sp) {
        bool ok = true;
#pragma unroll
        for (int k = 0; k < 4; ++k) ok &= ld_g(reinterpret_cast<const u64*>(ws + FLAG0) + 64 * k + lane) == tag64;
        if (__all(ok)) break;
        __builtin_amdgcn_s_sleep(8);
      }
      if (lane == 0) st_g(reinterpret_cast<u64*>(ws), tag64);
    }
    {
      for (unsigned sp = 0; ctl[14] < (unsigned)NCONS && sp < SPIN_CAP; ++sp) __builtin_amdgcn_s_sleep(4);
      SB();
      float M = -INFINITY, L = 0.f;
#pragma unroll
      for (int u = 0; u < NCONS; ++u) M = fmaxf(M, ml[2 * u]);
#pragma unroll
      for (int u = 0; u < NCONS; ++u) L = fmaf(__builtin_amdgcn_exp2f(ml[2 * u] - M), ml[2 * u + 1], L);
      if (lane == 0) st_g16(ws + GR0 + (size_t)GSTR * wg + 128, M, L, tag);
      const u32x4* g = ws + GR0 + (size_t)GSTR * (team0 + min(lane, T - 1)) + 128;
      u32x4 v;
      for (unsigned sp = 0; sp < SPIN_CAP; ++sp) {
        v = ld_g16(g);
        if (__all(gok(v, tag))) break;
        __builtin_amdgcn_s_sleep(4);
      }
      const float Mh = wave_max(__uint_as_float(v[0]));
      const float Lh = wave_sum(lane < T ? __builtin_amdgcn_exp2f(__uint_as_float(v[0]) - Mh) * __uint_as_float(v[2]) : 0.f);
      if (lane == 0) { ctl[16] = __float_as_uint(Mh); ctl[17] = __float_as_uint(1.f / Lh); }
      LGKM0();
      if (lane == 0) ctl[15] = 1u;
    }
    return;
  }
  const int ci = w - NLOAD - 1, ctid = ci * 64 + lane;
  const int r = lane >> 4, c = lane & 15;
  STAMP(0);
  LAS float* ap = (LAS float*)(L8 + AP_OFF);
  LAS float* yp = (LAS float*)L8;
  {
    const unsigned short* x = reinterpret_cast<const unsigned short*>(isA ? ax : bx);
    const __hip_bfloat16* wq = (isA ? awq : bwq) + h * DH + 8 * c;
    auto prologue = [&](auto nq_) {
      constexpr int NQ = decltype(nq_)::value;
      u32x4 wr[NQ]; unsigned xs[NQ];
      asm volatile("; prologue %0" :: "n"(NQ) : "memory");
#pragma unroll
      for (int i = 0; i < NQ; ++i) {
        const int row = qr0 + min(32 * i + 4 * ci + r, qcnt - 1);
        wr[i] = ldw(reinterpret_cast<const u32x4*>(wq + (size_t)row * (NH * DH)));
        xs[i] = x[row];
      }
      SB();
      BAR();
      SB();
      float qa[8];
#pragma unroll
      for (int k = 0; k < 8; ++k) qa[k] = 0.f;
#pragma unroll
      for (int i = 0; i < NQ; ++i) {
        asm volatile("" : "+v"(wr[i]), "+v"(xs[i]));
        const float xv = (32 * i + 4 * ci + r < qcnt) ? __uint_as_float(xs[i] << 16) : 0.f;
#pragma unroll
        for (int k = 0; k < 4; ++k) {
          qa[2 * k] = fmaf(xv, bf_lo(wr[i][k]), qa[2 * k]);
          qa[2 * k + 1] = fmaf(xv, bf_hi(wr[i][k]), qa[2 * k + 1]);
        }
      }
#pragma unroll
      for (int k = 0; k < 8; ++k) qa[k] = xrow_sum(qa[k]);
      if (r == 0) {
        *reinterpret_cast<LAS f32x4*>(qp + ci * DH + 8 * c) = f32x4{qa[0], qa[1], qa[2], qa[3]};
        *reinterpret_cast<LAS f32x4*>(qp + ci * DH + 8 * c + 4) = f32x4{qa[4], qa[5], qa[6], qa[7]};
      }
      __builtin_amdgcn_s_waitcnt(0);
      SB();
      if (lane == 0) __hip_atomic_fetch_add((LAS unsigned*)(ctl + 10), 1u, __ATOMIC_RELAXED, __HIP_MEMORY_SCOPE_WORKGROUP);
      SB();
    };
    if (isA) prologue(std::integral_constant<int, 4>{});
    else prologue(std::integral_constant<int, 10>{});
    SB();
  }
  const int G = (n + 15) >> 4;
  volatile LAS unsigned* full = ctl + 8 + (ci >> 2);
  LAS unsigned char* mine = L8 + ci * 1024 + lane * 16;
  int F = 0, s0r = 0;
#define FETCH4(R0, base, d)                                                                     \
    const int cnt_ = min(4, n - (R0)), need_ = (base) + (R0) + cnt_;                            \
    for (unsigned sp = 0; F < need_ && sp < SPIN_CAP; ++sp) { F = __builtin_amdgcn_readfirstlane(*full); if (F < need_) __builtin_amdgcn_s_sleep(1); } \
    asm volatile("" ::: "memory");                                                              \
    _Pragma("unroll") for (int t = 0; t < 4; ++t)                                               \
      (d)[t] = *reinterpret_cast<const LAS u32x4*>(mine + wrapr(s0r + min(t, cnt_ - 1)) * ROWB);  \
    LGKM0();                                                                                    \
    if (lane == 0) ctl[ci] = need_;                                                             \
    asm volatile("" : "+v"((d)[0]), "+v"((d)[1]), "+v"((d)[2]), "+v"((d)[3]) :: "memory");      \
    s0r = wrapr(s0r + cnt_);
  u32x4 q = {0u, 0u, 0u, 0u};
  float sc0 = -INFINITY, sc1 = -INFINITY, sc2 = -INFINITY;
#pragma unroll 1
  for (int g = 0; g < G; ++g) {
    u32x4 kd[16];
#pragma unroll
    for (int bb = 0; bb < 4; ++bb) {
      const int R0 = 16 * g + 4 * bb;
      if (R0 < n) { FETCH4(R0, 0, kd + 4 * bb) }
      else {
#pragma unroll
        for (int t = 0; t < 4; ++t) kd[4 * bb + t] = u32x4{0u, 0u, 0u, 0u};
      }
    }
    if (g == 0) {
      for (unsigned sp = 0; ctl[11] == 0u && sp < SPIN_CAP; ++sp) __builtin_amdgcn_s_sleep(1);
      SB();
      q = *reinterpret_cast<const LAS u32x4*>(L8 + QF_OFF + 16 * c);
    }
    float xx[16];
#pragma unroll
    for (int t = 0; t < 16; ++t) {
      float a = 0.f;
#pragma unroll
      for (int k = 0; k < 4; ++k) a = dot2(kd[t][k], q[k], a);
      xx[t] = a;
    }
    float v = transpose_reduce16(xx, c) * SC2;
    v = (16 * g + c < n) ? v : -INFINITY;
    sc2 = sc1; sc1 = sc0; sc0 = v;
  }
  const float m = wave_max(fmaxf(fmaxf(sc0, sc1), sc2));
  const float p0 = __builtin_amdgcn_exp2f(sc0 - m), p1 = __builtin_amdgcn_exp2f(sc1 - m), p2 = __builtin_amdgcn_exp2f(sc2 - m);
  const float l = wave_sum(p0 + p1 + p2);
  if (lane == 0) { ml[2 * ci] = m; ml[2 * ci + 1] = l; }
  LGKM0();
  if (lane == 0) __hip_atomic_fetch_add((LAS unsigned*)(ctl + 14), 1u, __ATOMIC_RELAXED, __HIP_MEMORY_SCOPE_WORKGROUP);
  STAMP(3);
  const __hip_bfloat16* wop = (isA ? awo : bwo) + (size_t)(h * DH + 16 * ci + 4 * r) * DM + 128 * t0 + 8 * c;
  u32x4 wor[3][4];
#pragma unroll
  for (int s = 0; s < 3; ++s) asm volatile("" : "=v"(wor[s][0]), "=v"(wor[s][1]), "=v"(wor[s][2]), "=v"(wor[s][3]));
#define WOLOAD(NT) do { _Pragma("unroll") for (int s = 0; s < (NT); ++s) { const int ss = min(s, nt - 1);        \
      _Pragma("unroll") for (int i = 0; i < 4; ++i)                                                              \
        wor[s][i] = ldw(reinterpret_cast<const u32x4*>(WOA(ss, i))); } SB(); } while (0)
#define WOA(ss, i) (wop + 128 * (ss) + (i) * DM)
  unsigned sel[2][4];
#pragma unroll
  for (int e = 0; e < 2; ++e)
#pragma unroll
    for (int a = 0; a < 4; ++a) {
      const unsigned pick = 0x0100u + 0x0202u * e;
      sel[e][a] = ((c >> 1) & 3) != a ? 0x0c0c0c0cu : (c & 1) ? (pick << 16) | 0x0c0cu : 0x0c0c0000u | pick;
      PIN(sel[e][a]);
    }
  u32x4* po = ws + GR0 + (size_t)GSTR * wg + 64 + 8 * ci + (lane & 7);
  const u32x4* pg = ws + GR0 + (size_t)GSTR * (team0 + min(c & 7, T - 1)) + 64 + 8 * ci + 2 * r;
  u32x4* py = ws + Y0 + (size_t)((isA ? 0 : NH) + h) * (DM / 2) + 64 * t0 + ctid;
  const bool red = ci == NCONS - 1 && b < 160;
  if (red) py = ws + Y0 + (size_t)(NH * (b / 80) + (lane >> 2)) * (DM / 2) + 4 * (b % 80) + (lane & 3);
  LAS float* la = ap + ci * APS + 8 * c + 2 * r;
  const LAS float* lb = ap + 4 * ((lane >> 3) & 1) * APS + 16 * ci + 2 * (lane & 7);
  LAS float* yw = yp + ci * YPS + 8 * c + 4 * (r & 1) + 2 * (r >> 1);
  PIN(po); PIN(pg); PIN(py); PIN(la); PIN(lb); PIN(yw);
  u32x2 tag = {0u, 0u};
  float f = 0.f;
#define HEAD() do { for (unsigned sp = 0; ctl[15] == 0u && sp < SPIN_CAP; ++sp) __builtin_amdgcn_s_sleep(1);  \
    SB(); tag = u32x2{ctl[12], ctl[13]};                                                                       \
    f = __builtin_amdgcn_exp2f(m - __uint_as_float(ctl[16])) * __uint_as_float(ctl[17]); SB(); } while (0)
  float acc[8];
#pragma unroll
  for (int k = 0; k < 8; ++k) acc[k] = 0.f;
  const int nf = (n - 1) & ~3;
  float pl[4];
#pragma unroll
  for (int t = 0; t < 4; ++t) { pl[t] = __shfl(p0, (lane & 48) | ((nf + t) & 15)); PIN(pl[t]); }
#pragma unroll 1
  for (int g = 0; 16 * g < nf; ++g) {
    const int back = G - 1 - g;
    const float p = back == 0 ? p0 : back == 1 ? p1 : p2;
#define PVB(BB) { const int R0 = 16 * g + 4 * (BB);                                             \
      if ((BB) == (WPA & 3) && isA && g == (WPA >> 2)) { if (nt && WLATE == 0) WOLOAD(1); HEAD(); }           \
      if ((BB) == (WPB & 3) && !isA && g == (WPB >> 2)) { WOLOAD(3); HEAD(); }                  \
      if (R0 < nf) { u32x4 d[4]; FETCH4(R0, n, d)                                               \
        pv_fma(d[0], bcast16<4 * (BB)>(p), acc); pv_fma(d[1], bcast16<4 * (BB) + 1>(p), acc);   \
        pv_fma(d[2], bcast16<4 * (BB) + 2>(p), acc); pv_fma(d[3], bcast16<4 * (BB) + 3>(p), acc); } }
    PVB(0) PVB(1) PVB(2) PVB(3)
#undef PVB
  }
#pragma unroll
  for (int t = 0; t < 4; ++t) {
    if (nf + t < n) {
      const LAS u32x4* a_ = reinterpret_cast<const LAS u32x4*>(mine + wrapr(s0r + t) * ROWB);
      PIN(a_);
      for (unsigned sp = 0; F <= n + nf + t && sp < SPIN_CAP; ++sp) F = __builtin_amdgcn_readfirstlane(*full);
      asm volatile("" ::: "memory");
      pv_fma(*a_, pl[t], acc);
      if (WLATE == 3 && t == 0 && isA && nt) WOLOAD(1);
    }
  }
  if (WLATE == 1 && isA && nt) WOLOAD(1);
  STAMP(4);
  {
    float t[4];
#pragma unroll
    for (int k = 0; k < 4; ++k) t[k] = fold32(acc[k], acc[k + 4]);
    *reinterpret_cast<LAS f32x2*>(la) = f32x2{fold16(t[0], t[2]) * f, fold16(t[1], t[3]) * f};
  }
  BAR();
  {
    f32x2 x = *reinterpret_cast<const LAS f32x2*>(lb);
#pragma unroll
    for (int v = 1; v < 4; ++v) x += *reinterpret_cast<const LAS f32x2*>(lb + v * APS);
    const float x0 = x[0] + dpp<XOR8>(x[0]), x1 = x[1] + dpp<XOR8>(x[1]);
    if (lane < 8) st_g16(po, x0, x1, tag);
  }
  if (WLATE == 2 && isA && nt) WOLOAD(1);
  STAMP(5);
  if (nt) {
    {
      u32x4 v0, v1;
      for (unsigned sp = 0; sp < SPIN_CAP; ++sp) {
        ld_g16p(pg, v0, v1);
        if (__all(gok(v0, tag) && gok(v1, tag))) break;
        __builtin_amdgcn_s_sleep(1);
      }
      STAMP(6);
      const bool on = (c & 7) < T;
      float o[4] = {on ? __uint_as_float(v0[0]) : 0.f, on ? __uint_as_float(v0[2]) : 0.f, on ? __uint_as_float(v1[0]) : 0.f, on ? __uint_as_float(v1[2]) : 0.f};
#pragma unroll
      for (int i = 0; i < 4; ++i) o[i] += dpp<XOR1>(o[i]);
#pragma unroll
      for (int i = 0; i < 4; ++i) o[i] += dpp<XOR2>(o[i]);
#pragma unroll
      for (int i = 0; i < 4; ++i) o[i] += dpp<XOR7>(o[i]);
      u32x4 af[4];
#pragma unroll
      for (int i = 0; i < 4; i += 2) {
        const unsigned hi = pack_bf16(o[i], o[i + 1]), lo = pack_bf16(o[i] - bf_lo(hi), o[i + 1] - bf_hi(hi));
        const unsigned pr = (c & 8) ? lo : hi;
#pragma unroll
        for (int a = 0; a < 4; ++a) { af[i][a] = __builtin_amdgcn_perm(0u, pr, sel[0][a]); af[i + 1][a] = __builtin_amdgcn_perm(0u, pr, sel[1][a]); }
      }
      auto tile = [&](int s) {
        f32x4 d = {0.f, 0.f, 0.f, 0.f};
#pragma unroll
        for (int i = 0; i < 4; ++i)
          d = __builtin_amdgcn_mfma_f32_16x16x32_bf16(__builtin_bit_cast(bf16x8, af[i]), __builtin_bit_cast(bf16x8, wor[s][i]), d, 0, 0, 0);
        *reinterpret_cast<LAS f32x2*>(yw + 128 * s) = f32x2{fold32(d[0], d[2]), fold32(d[1], d[3])};
      };
      tile(0);
      if (!isA) { tile(1); tile(2); }
    }
    BAR();
    if (ci < nt) {
      f32x2 y = *reinterpret_cast<const LAS f32x2*>(yp + 2 * ctid);
#pragma unroll
      for (int u = 1; u < NCONS; ++u) y += *reinterpret_cast<const LAS f32x2*>(yp + u * YPS + 2 * ctid);
      st_g16(py, y[0], y[1], tag);
    }
  }
  if (red) {
    u32x4 v0, v1;
    for (unsigned sp = 0; sp < SPIN_CAP; ++sp) {
      ld_g16x2(py, py + (size_t)16 * (DM / 2), v0, v1);
      if (__all(gok(v0, tag) && gok(v1, tag))) break;
      __builtin_amdgcn_s_sleep(1);
    }
    float s0 = __uint_as_float(v0[0]) + __uint_as_float(v1[0]), s1 = __uint_as_float(v0[2]) + __uint_as_float(v1[2]);
    s0 += dpp<0x124>(s0); s1 += dpp<0x124>(s1);
    s0 += dpp<XOR8>(s0); s1 += dpp<XOR8>(s1);
    s0 = xrow_sum(s0); s1 = xrow_sum(s1);
    if (lane < 4) reinterpret_cast<unsigned*>(out + 8 * b)[lane] = pack_bf16(s0, s1);
  }
  STAMP(7);
}

extern "C" void kernel_launch(void* const* d_in, const int* in_sizes, int n_in,
                              void* d_out, int out_size, void* d_ws, size_t ws_size,
                              hipStream_t stream) {
  typedef const __hip_bfloat16* P;
#define XA
  attn_main<<<NWG, NTHR, 0, stream>>>((P)d_in[0], (P)d_in[1], (P)d_in[2], (P)d_in[3], (P)d_in[4],
                                      (P)d_in[5], (P)d_in[6], (P)d_in[7], (P)d_in[8], (P)d_in[9],
                                      (u32x4*)d_ws, (__hip_bfloat16*)d_out XA);
}
```

```cpp
#include <hip/hip_runtime.h>
#include <hip/hip_bf16.h>
#include <stdint.h>

typedef unsigned u32x4 __attribute__((ext_vector_type(4)));
typedef unsigned u32x2 __attribute__((ext_vector_type(2)));
typedef unsigned u32x8 __attribute__((ext_vector_type(8)));
typedef float f32x4 __attribute__((ext_vector_type(4)));
typedef float f32x2 __attribute__((ext_vector_type(2)));
typedef __bf16 bf16x2 __attribute__((ext_vector_type(2)));
typedef __bf16 bf16x8 __attribute__((ext_vector_type(8)));
#define LAS __attribute__((address_space(3)))

constexpr int DM = 640, NH = 32, DH = 128, SK = 8192, WIN = 2048;
typedef unsigned long long u64;
constexpr int NCONS = 8, NLOAD = 2, NTHR = (NCONS + NLOAD + 1) * 64;
constexpr int SPLIT_A = 6, SPLIT_B = 2;
constexpr int NWG_A = NH * SPLIT_A, NWG_B = NH * SPLIT_B, NWG = NWG_A + NWG_B;
constexpr float SCL = 0.088388347648318440f, LOG2E = 1.4426950408889634f;
constexpr int RR = 18;
constexpr int IR = 6;
constexpr int R00 = 5;
constexpr int ROWB = 8192;
constexpr int TT = 24600, C0 = -700;
constexpr int FL = -600, KN = 6, D6 = -2000, D7 = -1300, DMAX = 2000;
constexpr int WPA = 10, WPB = 7;
constexpr unsigned SPIN_CAP = 1u << 20;
constexpr int APS = 132, YPS = 384;
constexpr int QP_OFF = RR * ROWB, AP_OFF = QP_OFF + 4096, ML_OFF = AP_OFF + NCONS * APS * 4, QF_OFF = ML_OFF + 64, CT_OFF = QF_OFF + 256, LDS_BYTES = CT_OFF + 128;
static_assert(LDS_BYTES <= 163840 && RR * ROWB >= NCONS * YPS * 4 && ML_OFF % 16 == 0, "LDS");
constexpr int FLAG0 = 64, TS0 = 192, GR0 = 512, GSTR = 136, Y0 = GR0 + GSTR * NWG;
#define SB() do { asm volatile("" ::: "memory"); __builtin_amdgcn_sched_barrier(0); } while (0)
#define PIN(x) asm volatile("" : "+v"(x))
#define LGKM0() asm volatile("s_waitcnt lgkmcnt(0)" ::: "memory")
#define BAR() asm volatile("s_waitcnt lgkmcnt(0)\n\ts_barrier" ::: "memory")

__device__ __forceinline__ float dot2(unsigned k, unsigned q, float acc) {
  return __builtin_amdgcn_fdot2_f32_bf16(__builtin_bit_cast(bf16x2, k), __builtin_bit_cast(bf16x2, q), acc, false);
}
template <int CTRL> __device__ __forceinline__ float dpp(float x) {
  return __builtin_bit_cast(float, __builtin_amdgcn_mov_dpp(__builtin_bit_cast(int, x), CTRL, 0xf, 0xf, true));
}
constexpr int XOR1 = 0xB1, XOR2 = 0x4E, XOR7 = 0x141, XOR8 = 0x128;
template <int T> __device__ __forceinline__ float bcast16(float p) { return dpp<0x150 + T>(p); }
__device__ __forceinline__ float xrow_max(float x) {
  auto s = __builtin_amdgcn_permlane16_swap(__float_as_uint(x), __float_as_uint(x), false, false);
  x = fmaxf(__uint_as_float(s[0]), __uint_as_float(s[1]));
  auto t = __builtin_amdgcn_permlane32_swap(__float_as_uint(x), __float_as_uint(x), false, false);
  return fmaxf(__uint_as_float(t[0]), __uint_as_float(t[1]));
}
__device__ __forceinline__ float xrow_sum(float x) {
  auto s = __builtin_amdgcn_permlane16_swap(__float_as_uint(x), __float_as_uint(x), false, false);
  x = __uint_as_float(s[0]) + __uint_as_float(s[1]);
  auto t = __builtin_amdgcn_permlane32_swap(__float_as_uint(x), __float_as_uint(x), false, false);
  return __uint_as_float(t[0]) + __uint_as_float(t[1]);
}
__device__ __forceinline__ float wave_max(float x) {
  x = fmaxf(x, dpp<XOR1>(x)); x = fmaxf(x, dpp<XOR2>(x)); x = fmaxf(x, dpp<XOR7>(x)); x = fmaxf(x, dpp<XOR8>(x));
  return xrow_max(x);
}
__device__ __forceinline__ float wave_sum(float x) {
  x += dpp<XOR1>(x); x += dpp<XOR2>(x); x += dpp<XOR7>(x); x += dpp<XOR8>(x);
  return xrow_sum(x);
}
__device__ __forceinline__ float bf_lo(unsigned u) { return __uint_as_float(u << 16); }
__device__ __forceinline__ float bf_hi(unsigned u) { return __uint_as_float(u & 0xffff0000u); }
__device__ __forceinline__ unsigned pack_bf16(float lo, float hi) {
  unsigned r; asm("v_cvt_pk_bf16_f32 %0, %1, %2" : "=v"(r) : "v"(lo), "v"(hi)); return r;
}
__device__ __forceinline__ u64 ld_g(const u64* p) { return __hip_atomic_load(p, __ATOMIC_RELAXED, __HIP_MEMORY_SCOPE_AGENT); }
__device__ __forceinline__ void st_g(u64* p, u64 v) { __hip_atomic_store(p, v, __ATOMIC_RELAXED, __HIP_MEMORY_SCOPE_AGENT); }
__device__ __forceinline__ void st_g16(u32x4* p, float v0, float v1, u32x2 tag) {
  const u32x4 v = {__float_as_uint(v0), tag[0], __float_as_uint(v1), tag[1]};
  asm volatile("global_store_dwordx4 %0, %1, off sc1\n\ts_nop 1" :: "v"(p), "v"(v) : "memory");
}
__device__ __forceinline__ u32x4 ld_g16(const u32x4* p) {
  u32x4 a;
  asm volatile("global_load_dwordx4 %0, %1, off sc1\n\ts_waitcnt vmcnt(0)" : "=&v"(a) : "v"(p) : "memory");
  return a;
}
__device__ __forceinline__ float fold32(float x, float y) {
  auto s = __builtin_amdgcn_permlane32_swap(__float_as_uint(x), __float_as_uint(y), false, false);
  return __uint_as_float(s[0]) + __uint_as_float(s[1]);
}
__device__ __forceinline__ float fold16(float x, float y) {
  auto s = __builtin_amdgcn_permlane16_swap(__float_as_uint(x), __float_as_uint(y), false, false);
  return __uint_as_float(s[0]) + __uint_as_float(s[1]);
}
__device__ __forceinline__ void ld_g16x6(const u32x4* p, const int (&o)[6], u32x4 (&v)[6]) {
  asm volatile("global_load_dwordx4 %0, %6, off sc1\n\tglobal_load_dwordx4 %1, %7, off sc1\n\t"
               "global_load_dwordx4 %2, %8, off sc1\n\tglobal_load_dwordx4 %3, %9, off sc1\n\t"
               "global_load_dwordx4 %4, %10, off sc1\n\tglobal_load_dwordx4 %5, %11, off sc1\n\ts_waitcnt vmcnt(0)"
               : "=&v"(v[0]), "=&v"(v[1]), "=&v"(v[2]), "=&v"(v[3]), "=&v"(v[4]), "=&v"(v[5])
               : "v"(p + o[0]), "v"(p + o[1]), "v"(p + o[2]), "v"(p + o[3]), "v"(p + o[4]), "v"(p + o[5]) : "memory");
}
__device__ __forceinline__ bool gok(const u32x4 g, u32x2 tag) { return g[1] == tag[0] && g[3] == tag[1]; }
#define PSTEP(A, B, C, D, L)                                                                                  \
  "s_waitcnt vmcnt(2)\n\tv_cmp_ne_u32 vcc, v" #B ", %8\n\tv_cmp_ne_u32 %5, v" #D ", %9\n\ts_or_b64 vcc, vcc, %5\n\t"   \
  "s_cbranch_scc0 .Lp" #L "%=\n\ts_mov_b64 exec, vcc\n\tglobal_load_dwordx4 v[" #A ":" #D "], %7, off sc1\n\ts_mov_b64 exec, -1\n\t"
__device__ __forceinline__ void poll(const u32x4* p, u32x2 tag, u32x4 (&t)[3], float& v0, float& v1) {
  u64 m; unsigned cnt;
  asm volatile("global_load_dwordx4 v[136:139], %7, off sc1\n\ts_sleep %10\n\t"
               "global_load_dwordx4 v[140:143], %7, off sc1\n\ts_sleep %10\n\t"
               "global_load_dwordx4 v[144:147], %7, off sc1\n\ts_mov_b32 %6, 0\n"
               ".Lpl%=:\n\t"
               PSTEP(136, 137, 138, 139, a) PSTEP(140, 141, 142, 143, b) PSTEP(144, 145, 146, 147, c)
               "s_add_u32 %6, %6, 1\n\ts_cmp_lt_u32 %6, %11\n\ts_cbranch_scc1 .Lpl%=\n"
               ".Lpc%=:\n\tv_mov_b32 %0, v144\n\tv_mov_b32 %1, v146\n\ts_branch .Lpe%=\n"
               ".Lpb%=:\n\tv_mov_b32 %0, v140\n\tv_mov_b32 %1, v142\n\ts_branch .Lpe%=\n"
               ".Lpa%=:\n\tv_mov_b32 %0, v136\n\tv_mov_b32 %1, v138\n"
               ".Lpe%=:"
               : "=&v"(v0), "=&v"(v1), "={v[136:139]}"(t[0]), "={v[140:143]}"(t[1]), "={v[144:147]}"(t[2]), "=&s"(m), "=&s"(cnt)
               : "v"(p), "v"(tag[0]), "v"(tag[1]), "n"(8), "n"(SPIN_CAP) : "memory", "vcc", "scc");
}
#undef PSTEP
__device__ __forceinline__ void poll_drain(u32x4 (&t)[3]) {
  asm volatile("" : "+{v[136:139]}"(t[0]), "+{v[140:143]}"(t[1]), "+{v[144:147]}"(t[2]) :: "memory");
}

__device__ __forceinline__ float transpose_reduce16(float (&x)[16], int c) {
#define TR_STEP(HALF, CTRL, BIT)                                                          \
  _Pragma("unroll") for (int h = 0; h < (HALF); ++h) {                                    \
    const float lo_ = x[h], hi_ = x[h + (HALF)];                                          \
    const float t1_ = lo_ + dpp<CTRL>(lo_), t2_ = hi_ + dpp<CTRL>(hi_);                   \
    x[h] = (c & (BIT)) ? t2_ : t1_; }
  TR_STEP(8, XOR8, 8) TR_STEP(4, XOR7, 4) TR_STEP(2, XOR2, 2) TR_STEP(1, XOR1, 1)
#undef TR_STEP
  return x[0];
}
__device__ __forceinline__ void pv_fma(const u32x4 d, float pt, float (&acc)[8]) {
#pragma unroll
  for (int k = 0; k < 4; ++k) {
    acc[2 * k] = fmaf(pt, bf_lo(d[k]), acc[2 * k]);
    acc[2 * k + 1] = fmaf(pt, bf_hi(d[k]), acc[2 * k + 1]);
  }
}

__device__ __forceinline__ void dma4k(u64 base, unsigned voff, unsigned lds_dst) {
  unsigned keep;
  asm volatile("s_mov_b32 %0, m0\n\ts_mov_b32 m0, %3\n\ts_nop 0\n\t"
               "global_load_lds_dwordx4 %1, %2 nt\n\t"
               "global_load_lds_dwordx4 %1, %2 offset:1024 nt\n\t"
               "global_load_lds_dwordx4 %1, %2 offset:2048 nt\n\t"
               "global_load_lds_dwordx4 %1, %2 offset:3072 nt\n\t"
               "s_mov_b32 m0, %0" : "=&s"(keep) : "v"(voff), "s"(base), "s"(lds_dst) : "memory");
}
template <int N> __device__ __forceinline__ void vmw() { asm volatile("s_waitcnt vmcnt(%0)" :: "n"(N) : "memory"); }
__device__ __forceinline__ void vm_rows(int k) {
#define VE(N) "s_waitcnt vmcnt(" #N ")\n\ts_branch .Lv%=\n\t"
  asm volatile("s_getpc_b64 vcc\n\ts_add_u32 vcc_lo, vcc_lo, %0\n\ts_addc_u32 vcc_hi, vcc_hi, 0\n\ts_setpc_b64 vcc\n\t"
               VE(0) VE(4) VE(8) VE(12) VE(16) VE(20) VE(24) VE(28) VE(32) VE(36) VE(40) VE(44) VE(48) VE(52) VE(56) VE(60)
               ".Lv%=:" :: "s"(8 * k + 12) : "memory", "vcc", "scc");
#undef VE
}
template <int K> __device__ __forceinline__ void drain_ladder(volatile LAS unsigned* full, int total, int pub) {
  if constexpr (K >= 0) { vmw<4 * K>(); if (total - K > pub) *full = total - K; drain_ladder<K - 1>(full, total, pub); }
}
__device__ __forceinline__ int wrapr(int s) { return s >= RR ? s - RR : s; }

__global__ void __launch_bounds__(NTHR)
attn_main(const __hip_bfloat16* __restrict__ ax, const __hip_bfloat16* __restrict__ ak, const __hip_bfloat16* __restrict__ av,
          const __hip_bfloat16* __restrict__ awq, const __hip_bfloat16* __restrict__ awo,
          const __hip_bfloat16* __restrict__ bx, const __hip_bfloat16* __restrict__ bk, const __hip_bfloat16* __restrict__ bv,
          const __hip_bfloat16* __restrict__ bwq, const __hip_bfloat16* __restrict__ bwo,
          u32x4* ws_, __hip_bfloat16* out_) {
  __shared__ __attribute__((aligned(16))) unsigned char lds[LDS_BYTES];
  LAS unsigned char* L8 = (LAS unsigned char*)lds;
  volatile LAS unsigned* ctl = (volatile LAS unsigned*)(L8 + CT_OFF);
  const int lane = threadIdx.x & 63, w = __builtin_amdgcn_readfirstlane(threadIdx.x >> 6);
  unsigned k0, k1, z0, z1;
  asm volatile("s_cmp_eq_u32 %5, 2\n\ts_cbranch_scc1 .Lz%=\n\ts_load_dword %0, %4, 0\n\ts_load_dword %1, %4, 0x5c\n\ts_branch .Ly%=\n"
               ".Lz%=:\n\tv_and_b32 %2, 31, %6\n\tv_lshl_add_u32 %2, %2, 2, %7\n\tv_mov_b32 %3, 0\n\tds_write_b32 %2, %3\n\ts_waitcnt lgkmcnt(0)\n"
               ".Ly%=:"
               : "=&s"(k0), "=&s"(k1), "=&v"(z0), "=&v"(z1)
               : "s"(__builtin_amdgcn_kernarg_segment_ptr()), "s"(w), "v"(threadIdx.x), "s"((unsigned)(size_t)ctl) : "memory", "scc");
  SB();
  const int b = blockIdx.x, xcd = b & 7, bi = b >> 3;
  const int pos = (xcd + bi) & 7, h = bi;
  const bool isA = pos < SPLIT_A;
  u32x8 ka; u32x2 kb; u32x4 kc;
  asm volatile("s_load_dwordx8 %0, %3, %4\n\ts_load_dwordx2 %1, %3, %5\n\ts_load_dwordx4 %2, %3, 0x50"
               : "=&s"(ka), "=&s"(kb), "=&s"(kc)
               : "s"(__builtin_amdgcn_kernarg_segment_ptr()), "s"(isA ? 0 : 40), "s"(isA ? 32 : 72));
  SB();
#define TAB(v0, v1, v2, v3, v4, v5, v6, v7) \
  ((int)((((u64)(v7) << 56 | (u64)(v6) << 48 | (u64)(v5) << 40 | (u64)(v4) << 32 | (u64)(v3) << 24 | (u64)(v2) << 16 | (u64)(v1) << 8 | (u64)(v0)) >> (8 * pos)) & 255u))
  const int j = TAB(0, 1, 2, 3, 4, 5, 0, 1), T = TAB(6, 6, 6, 6, 6, 6, 2, 2);
#define NN 42, 42, 42, 43, 43, 44
#define NQ 4, 4, 4, 2, 2, 4
#define NW 1, 1, 1, 1, 1, 0
  constexpr int cn[8] = {NN, 31, 33}, cq[8] = {NQ, 10, 10}, cw[8] = {NW, 3, 2};
#define T8(a) TAB(a[0], a[1], a[2], a[3], a[4], a[5], a[6], a[7])
#define PRE(a, b6, b7) TAB(0, a[0], a[0] + a[1], a[0] + a[1] + a[2], a[0] + a[1] + a[2] + a[3], a[0] + a[1] + a[2] + a[3] + a[4], b6, b7)
  const int row0 = PRE(cn, 192, 223), n = T8(cn);
  const int qr0 = 32 * PRE(cq, 0, 10), nq = T8(cq);
  const int t0 = min(PRE(cw, 0, 3), 4), nt = T8(cw);
  const int team0 = h * T + TAB(0, 0, 0, 0, 0, 0, NWG_A, NWG_A);
#undef TAB
  const int wg = team0 + j;
  const int ci = w - NLOAD - 1, ctid = ci * 64 + lane;
  const int r = lane >> 4, c = lane & 15;
  unsigned off = (h * (SK / 32) + row0) * ROWB + w * 4096 + lane * 16;
  const unsigned lbase = (unsigned)(size_t)L8 + w * 4096;
  const unsigned qrow = qr0 + 4 * ci + r;
  unsigned wqo = qrow * (NH * DH * 2) + h * (DH * 2) + 16 * c;
  if (w < NLOAD) wqo = off;
  if (w == NLOAD) wqo = 0u;
  PIN(off); PIN(wqo);
  SB();
#define PTR(v, i) ((u64)(v)[(i) + 1] << 32 | (u64)(v)[i])
#define GPTR(type, v, i) ((type*)(__attribute__((address_space(1))) type*)PTR(v, i))
  u32x4 wr[10]; unsigned xs[10];
  u64 ep, ta64;
#define QL(W, X, O) "global_load_dwordx4 %" #W ", %20, %23 nt\n\tglobal_load_ushort %" #X ", %19, %24 offset:" #O "\n\tv_add_u32 %20, 0x40000, %20\n\t"
#define KL "global_load_lds_dwordx4 %20, %27 nt\n\tglobal_load_lds_dwordx4 %20, %27 offset:1024 nt\n\t"          \
           "global_load_lds_dwordx4 %20, %27 offset:2048 nt\n\tglobal_load_lds_dwordx4 %20, %27 offset:3072 nt\n\t"
#define KN_ "s_add_u32 m0, m0, 0x2000\n\tv_add_u32 %20, 0x2000, %20\n\t"
  static_assert(R00 == 5 && ROWB == 0x2000, "prologue");
  asm volatile("s_cmp_gt_u32 %25, 0\n\ts_cbranch_scc1 .Lo%=\n\t"
               "s_mov_b32 vcc_lo, m0\n\ts_mov_b32 m0, %26\n\ts_waitcnt lgkmcnt(0)\n\ts_memrealtime %22\n\t"
               KL KN_ KL KN_ KL KN_ KL KN_ KL
               "s_mov_b32 m0, vcc_lo\n\ts_barrier\n\ts_branch .Lq%=\n"
               ".Lo%=:\n\ts_cmp_gt_u32 %25, 1\n\ts_cbranch_scc1 .Lw%=\n\t"
               "s_waitcnt lgkmcnt(0)\n\ts_memrealtime %22\n\tglobal_load_dwordx2 %21, %20, %28 sc1\n\ts_barrier\n\ts_branch .Lq%=\n"
               ".Lw%=:\n\tv_lshrrev_b32 %19, 13, %20\n\tv_lshlrev_b32 %19, 1, %19\n\ts_barrier\n\ts_waitcnt lgkmcnt(0)\n\t"
               QL(0, 10, 0) QL(1, 11, 64)
               "s_cmp_lt_u32 %25, 3\n\ts_cbranch_scc1 .Lq%=\n\t"
               QL(2, 12, 128) QL(3, 13, 192)
               "s_cmp_lt_u32 %25, 5\n\ts_cbranch_scc1 .Lq%=\n\t"
               QL(4, 14, 256) QL(5, 15, 320) QL(6, 16, 384) QL(7, 17, 448) QL(8, 18, 512) QL(9, 19, 576)
               ".Lq%=:\n\ts_waitcnt lgkmcnt(0)"
               : "=&v"(wr[0]), "=&v"(wr[1]), "=&v"(wr[2]), "=&v"(wr[3]), "=&v"(wr[4]), "=&v"(wr[5]), "=&v"(wr[6]), "=&v"(wr[7]), "=&v"(wr[8]), "=&v"(wr[9]),
                 "=&v"(xs[0]), "=&v"(xs[1]), "=&v"(xs[2]), "=&v"(xs[3]), "=&v"(xs[4]), "=&v"(xs[5]), "=&v"(xs[6]), "=&v"(xs[7]), "=&v"(xs[8]), "=&v"(xs[9]),
                 "+v"(wqo), "=&v"(ep), "=&s"(ta64)
               : "s"(PTR(ka, 6)), "s"(PTR(ka, 0)), "s"(w < NLOAD ? 0 : w == NLOAD ? 1 : nq), "s"(lbase), "s"(PTR(ka, 2)), "s"(PTR(kc, 0))
               : "memory", "scc", "vcc");
#undef QL
#undef KL
#undef KN_
  asm volatile("" : "+s"(ka), "+s"(kb), "+s"(kc), "+s"(k0), "+s"(k1));
  SB();
  unsigned ta = (unsigned)ta64;
  u32x4* ws = GPTR(u32x4, kc, 0);
  SB();
  if (__builtin_expect(w < NLOAD, 1)) {
    const u64 kp = PTR(ka, 2), vp = PTR(ka, 4) - (u64)n * ROWB;
    volatile LAS unsigned* full = ctl + 8 + w;
    int R = R00, slot = R00, lim = RR, pub = 0;
#define TQ(p, d) (8 * (TT - FL + (d)) / (cq[p] + 2 * cn[p] - 1))
    const int tau = pos == 0 ? TQ(0, 0) : pos == 1 ? TQ(1, 0) : pos == 2 ? TQ(2, 0) : pos == 3 ? TQ(3, 0) : pos == 4 ? TQ(4, 0) : pos == 5 ? TQ(5, 0) : pos == 6 ? TQ(6, D6) : TQ(7, D7);
    int due = 8 * C0 + (nq + R00) * tau + 1024 * 80;
#pragma unroll 1
    for (unsigned sp = 0; R < 2 * n && sp < 16u * SPIN_CAP; ++sp) {
      const unsigned tn = (unsigned)__builtin_amdgcn_s_memrealtime();
      unsigned st;
      asm volatile("s_getreg_b32 %0, hwreg(HW_REG_IB_STS)" : "=s"(st) :: "memory");
      const int k = (int)(((st & 15u) | ((st >> 18) & 48u)) + 3u) >> 2;
      if (R - k > pub) { vm_rows(k); pub = R - k; *full = pub; }
      if ((int)min(tn - ta + 1024u, 1u << 20) * 80 >= due && k < IR) {
        if (R >= lim) {
          const u32x4 f = *(volatile LAS u32x4*)(ctl + 4 * w);
          lim = __builtin_amdgcn_readfirstlane(min(min(f[0], f[1]), min(f[2], f[3]))) + RR;
          asm volatile("" ::: "memory");
        }
        if (R < lim) {
          dma4k(R < n ? kp : vp, off + R * ROWB, lbase + slot * ROWB);
          slot = wrapr(slot + 1); ++R; due += tau + (R >= 2 * n - KN ? 8 * FL / KN : 0);
          continue;
        }
      }
      if (R < 24) { const unsigned a = __builtin_amdgcn_readfirstlane(ctl[18]); if (a) ta = a; }
      __builtin_amdgcn_s_sleep(1);
    }
    drain_ladder<IR - 1>(full, 2 * n, pub);
    return;
  }
  LAS float* qp = (LAS float*)(L8 + QP_OFF);
  LAS float* ml = (LAS float*)(L8 + ML_OFF);
  if (w == NLOAD) {
    asm volatile("s_waitcnt vmcnt(0)" : "+v"(ep) :: "memory");
    const u64 tag64 = ep + 1ull;
    const u32x2 tag = {(unsigned)tag64, (unsigned)(tag64 >> 32) ^ ((unsigned)tag64 * 0x9E3779B1u)};
    if (lane == 0) {
      st_g16(ws + TS0 + b, __uint_as_float(ta), 0.f, tag);
      st_g(reinterpret_cast<u64*>(ws + FLAG0) + wg, tag64);
    }
    if (lane < 2) ctl[12 + lane] = tag[lane];
#define LEAD() do { const u32x4 v = ld_g16(ws + TS0 + (b & ~7) + (lane & 7));                            \
      const bool ok = gok(v, tag) || (lane & 7) == (b & 7);                                               \
      float d = gok(v, tag) ? fminf(fmaxf((float)(int)(ta - v[0]), (float)(-DMAX / 10)), (float)(DMAX / 10)) : 0.f, e = ok ? 1.f : 0.f; \
      d += dpp<XOR1>(d); d += dpp<XOR2>(d); d += dpp<XOR7>(d); e += dpp<XOR1>(e); e += dpp<XOR2>(e); e += dpp<XOR7>(e); d /= e; \
      if (lane == 0) ctl[18] = (ta - (unsigned)(int)d) | 1u; } while (0)
    LEAD();
    for (unsigned sp = 0; ctl[10] < (unsigned)NCONS && sp < SPIN_CAP; ++sp) __builtin_amdgcn_s_sleep(1);
    SB();
    {
      float s0 = 0.f, s1 = 0.f;
#pragma unroll
      for (int u = 0; u < NCONS; ++u) { s0 += qp[u * DH + 2 * lane]; s1 += qp[u * DH + 2 * lane + 1]; }
      st_g16(ws + GR0 + (size_t)GSTR * wg + lane, s0, s1, tag);
    }
    {
      const u32x4* g = ws + GR0 + (size_t)GSTR * team0 + lane;
      int o[6];
#pragma unroll
      for (int u = 0; u < SPLIT_A; ++u) o[u] = GSTR * min(u, T - 1);
      u32x4 v[6];
      __builtin_amdgcn_s_sleep(0);
      {
        const u32x4* g1 = ws + GR0 + (size_t)GSTR * (team0 + min(lane & 7, T - 1)) + 63;
        for (unsigned sp = 0; sp < SPIN_CAP; ++sp) {
          u32x4 z = {0u, tag[0], 0u, tag[1]};
          if (lane < 8) z = ld_g16(g1);
          if (__all(gok(z, tag))) break;
          __builtin_amdgcn_s_sleep(1);
        }
      }
      for (unsigned sp = 0; sp < SPIN_CAP; ++sp) {
        ld_g16x6(g, o, v);
        bool ok = true;
#pragma unroll
        for (int u = 0; u < SPLIT_A; ++u) ok &= gok(v[u], tag);
        if (__all(ok)) break;
        __builtin_amdgcn_s_sleep(1);
      }
      float s0 = 0.f, s1 = 0.f;
#pragma unroll
      for (int u = 0; u < SPLIT_A; ++u) if (u < T) { s0 += __uint_as_float(v[u][0]); s1 += __uint_as_float(v[u][2]); }
      ((LAS unsigned*)(L8 + QF_OFF))[lane] = pack_bf16(s0, s1);
      LGKM0();
      if (lane == 0) ctl[11] = 1u;
    }
    LEAD();
    if (b == 0) {
      for (unsigned sp = 0; sp < SPIN_CAP; ++sp) {
        bool ok = true;
#pragma unroll
        for (int k = 0; k < 4; ++k) ok &= ld_g(reinterpret_cast<const u64*>(ws + FLAG0) + 64 * k + lane) == tag64;
        if (__all(ok)) break;
        __builtin_amdgcn_s_sleep(8);
      }
      if (lane == 0) st_g(reinterpret_cast<u64*>(ws), tag64);
    }
    {
      for (unsigned sp = 0; ctl[14] < (unsigned)NCONS && sp < SPIN_CAP; ++sp) __builtin_amdgcn_s_sleep(4);
      SB();
      float M = -INFINITY, L = 0.f;
#pragma unroll
      for (int u = 0; u < NCONS; ++u) M = fmaxf(M, ml[2 * u]);
#pragma unroll
      for (int u = 0; u < NCONS; ++u) L = fmaf(__builtin_amdgcn_exp2f(ml[2 * u] - M), ml[2 * u + 1], L);
      if (lane == 0) st_g16(ws + GR0 + (size_t)GSTR * wg + 128, M, L, tag);
      const u32x4* g = ws + GR0 + (size_t)GSTR * (team0 + min(lane, T - 1)) + 128;
      u32x4 v;
      for (unsigned sp = 0; sp < SPIN_CAP; ++sp) {
        __builtin_amdgcn_s_sleep(4);
        v = ld_g16(g);
        if (__all(gok(v, tag))) break;
      }
      const float Mh = wave_max(__uint_as_float(v[0]));
      const float Lh = wave_sum(lane < T ? __builtin_amdgcn_exp2f(__uint_as_float(v[0]) - Mh) * __uint_as_float(v[2]) : 0.f);
      if (lane == 0) { ctl[16] = __float_as_uint(Mh); ctl[17] = __float_as_uint(1.f / Lh); }
      LGKM0();
      if (lane == 0) ctl[15] = 1u;
    }
    return;
  }
  LAS float* ap = (LAS float*)(L8 + AP_OFF);
  LAS float* yp = (LAS float*)L8;
  {
    asm volatile("s_waitcnt vmcnt(0)"
                 : "+v"(wr[0]), "+v"(wr[1]), "+v"(wr[2]), "+v"(wr[3]), "+v"(wr[4]), "+v"(wr[5]), "+v"(wr[6]), "+v"(wr[7]), "+v"(wr[8]), "+v"(wr[9]),
                   "+v"(xs[0]), "+v"(xs[1]), "+v"(xs[2]), "+v"(xs[3]), "+v"(xs[4]), "+v"(xs[5]), "+v"(xs[6]), "+v"(xs[7]), "+v"(xs[8]), "+v"(xs[9]) :: "memory");
    float qa[8];
#pragma unroll
    for (int k = 0; k < 8; ++k) qa[k] = 0.f;
#pragma unroll
    for (int i = 0; i < 10; ++i) {
      if ((i == 2 || i == 4) && i >= nq) break;
      const float xv = __uint_as_float(xs[i] << 16);
#pragma unroll
      for (int k = 0; k < 4; ++k) {
        qa[2 * k] = fmaf(xv, bf_lo(wr[i][k]), qa[2 * k]);
        qa[2 * k + 1] = fmaf(xv, bf_hi(wr[i][k]), qa[2 * k + 1]);
      }
    }
#pragma unroll
    for (int k = 0; k < 8; ++k) qa[k] = xrow_sum(qa[k]);
    if (r == 0) {
      *reinterpret_cast<LAS f32x4*>(qp + ci * DH + 8 * c) = f32x4{qa[0], qa[1], qa[2], qa[3]};
      *reinterpret_cast<LAS f32x4*>(qp + ci * DH + 8 * c + 4) = f32x4{qa[4], qa[5], qa[6], qa[7]};
    }
    __builtin_amdgcn_s_waitcnt(0);
    SB();
    if (lane == 0) __hip_atomic_fetch_add((LAS unsigned*)(ctl + 10), 1u, __ATOMIC_RELAXED, __HIP_MEMORY_SCOPE_WORKGROUP);
    SB();
  }
  const int G = (n + 15) >> 4;
  volatile LAS unsigned* full = ctl + 8 + (ci >> 2);
  LAS unsigned char* mine = L8 + ci * 1024 + lane * 16;
  int F = 0, s0r = 0;
#define FETCH4(R0, base, d)                                                                     \
    const int cnt_ = min(4, n - (R0)), need_ = (base) + (R0) + cnt_;                            \
    for (unsigned sp = 0; F < need_ && sp < SPIN_CAP; ++sp) { F = __builtin_amdgcn_readfirstlane(*full); if (F < need_) __builtin_amdgcn_s_sleep(1); } \
    asm volatile("" ::: "memory");                                                              \
    _Pragma("unroll") for (int t = 0; t < 4; ++t)                                               \
      (d)[t] = *reinterpret_cast<const LAS u32x4*>(mine + wrapr(s0r + min(t, cnt_ - 1)) * ROWB);  \
    LGKM0();                                                                                    \
    if (lane == 0) ctl[ci] = need_;                                                             \
    asm volatile("" : "+v"((d)[0]), "+v"((d)[1]), "+v"((d)[2]), "+v"((d)[3]) :: "memory");      \
    s0r = wrapr(s0r + cnt_);
  u32x4 q = {0u, 0u, 0u, 0u};
  float sc0 = -INFINITY, sc1 = -INFINITY, sc2 = -INFINITY;
#pragma unroll 1
  for (int g = 0; g < G; ++g) {
    u32x4 kd[16];
#pragma unroll
    for (int bb = 0; bb < 4; ++bb) {
      const int R0 = 16 * g + 4 * bb;
      if (R0 < n) { FETCH4(R0, 0, kd + 4 * bb) }
      else {
#pragma unroll
        for (int t = 0; t < 4; ++t) kd[4 * bb + t] = u32x4{0u, 0u, 0u, 0u};
      }
    }
    if (g == 0) {
      for (unsigned sp = 0; ctl[11] == 0u && sp < SPIN_CAP; ++sp) __builtin_amdgcn_s_sleep(1);
      SB();
      q = *reinterpret_cast<const LAS u32x4*>(L8 + QF_OFF + 16 * c);
    }
    float xx[16];
#pragma unroll
    for (int t = 0; t < 16; ++t) {
      float a = 0.f;
#pragma unroll
      for (int k = 0; k < 4; ++k) a = dot2(kd[t][k], q[k], a);
      xx[t] = a;
    }
    float v = bf_lo(pack_bf16(transpose_reduce16(xx, c), 0.f));
    v = bf_lo(pack_bf16(v * SCL, 0.f)) * LOG2E;
    v = (16 * g + c < n) ? v : -INFINITY;
    sc2 = sc1; sc1 = sc0; sc0 = v;
  }
  const float m = wave_max(fmaxf(fmaxf(sc0, sc1), sc2));
  const float p0 = __builtin_amdgcn_exp2f(sc0 - m), p1 = __builtin_amdgcn_exp2f(sc1 - m), p2 = __builtin_amdgcn_exp2f(sc2 - m);
  const float l = wave_sum(p0 + p1 + p2);
  if (lane == 0) { ml[2 * ci] = m; ml[2 * ci + 1] = l; }
  LGKM0();
  if (lane == 0) __hip_atomic_fetch_add((LAS unsigned*)(ctl + 14), 1u, __ATOMIC_RELAXED, __HIP_MEMORY_SCOPE_WORKGROUP);
  const unsigned short* wop = GPTR(const unsigned short, kb, 0) + (size_t)(h * DH + 16 * ci + 4 * r) * DM + 128 * t0 + 8 * c;
  u32x4 wor[3][4];
#pragma unroll
  for (int s = 0; s < 3; ++s) asm volatile("" : "=v"(wor[s][0]), "=v"(wor[s][1]), "=v"(wor[s][2]), "=v"(wor[s][3]));
#define WT(A, B, C, D, O0, O1, O2, O3, N) "s_cmp_lt_i32 %13, " #N "\n\ts_cbranch_scc1 .Lw%=\n\t"                                   \
  "global_load_dwordx4 %" #A ", %12, off offset:" #O0 " nt\n\tglobal_load_dwordx4 %" #B ", %12, off offset:" #O1 " nt\n\t"     \
  "global_load_dwordx4 %" #C ", %12, off offset:" #O2 " nt\n\tglobal_load_dwordx4 %" #D ", %12, off offset:" #O3 " nt\n\t"
#define WOASM(cnt) do { asm volatile(WT(0, 1, 2, 3, -1920, -640, 640, 1920, 1) WT(4, 5, 6, 7, -1664, -384, 896, 2176, 2)          \
                                     WT(8, 9, 10, 11, -1408, -128, 1152, 2432, 3) ".Lw%=:"                                       \
      : "+v"(wor[0][0]), "+v"(wor[0][1]), "+v"(wor[0][2]), "+v"(wor[0][3]), "+v"(wor[1][0]), "+v"(wor[1][1]), "+v"(wor[1][2]),    \
        "+v"(wor[1][3]), "+v"(wor[2][0]), "+v"(wor[2][1]), "+v"(wor[2][2]), "+v"(wor[2][3])                                      \
      : "v"(wop + 960), "s"(cnt) : "memory", "scc"); SB(); } while (0)
  unsigned sel[2][4];
#pragma unroll
  for (int e = 0; e < 2; ++e)
#pragma unroll
    for (int a = 0; a < 4; ++a) {
      const unsigned pick = 0x0100u + 0x0202u * e;
      sel[e][a] = ((c >> 1) & 3) != a ? 0x0c0c0c0cu : (c & 1) ? (pick << 16) | 0x0c0cu : 0x0c0c0000u | pick;
      PIN(sel[e][a]);
    }
  u32x4* po = ws + GR0 + (size_t)GSTR * wg + 64 + 8 * ci + (lane & 7);
  const u32x4* pg = ws + GR0 + (size_t)GSTR * (team0 + min(c & 7, T - 1)) + 64 + 8 * ci + 2 * r + (c >> 3);
  u32x4* py = ws + Y0 + (size_t)((isA ? 0 : NH) + h) * (DM / 2) + 64 * t0 + ctid;
  const bool red = ci >= NCONS - 2 && b < 160;
  const int rb = 2 * b + (ci & 1);
  if (red) py = ws + Y0 + (size_t)(NH * (b / 80) + (lane >> 1)) * (DM / 2) + 2 * (rb % 160) + (lane & 1);
  LAS float* la = ap + ci * APS + 8 * c + 2 * r;
  const LAS float* lb = ap + 4 * ((lane >> 3) & 1) * APS + 16 * ci + 2 * (lane & 7);
  LAS float* yw = yp + ci * YPS + 8 * c + 4 * (r & 1) + 2 * (r >> 1);
  PIN(po); PIN(pg); PIN(py); PIN(la); PIN(lb); PIN(yw);
  u32x2 tag = {0u, 0u};
  float f = 0.f;
#define HEAD() do { for (unsigned sp = 0; ctl[15] == 0u && sp < SPIN_CAP; ++sp) __builtin_amdgcn_s_sleep(1);  \
    SB(); tag = u32x2{ctl[12], ctl[13]};                                                                       \
    f = __builtin_amdgcn_exp2f(m - __uint_as_float(ctl[16])) * __uint_as_float(ctl[17]); SB(); } while (0)
  float acc[8];
#pragma unroll
  for (int k = 0; k < 8; ++k) acc[k] = 0.f;
  const int nf = (n - 1) & ~3;
  float pl[4];
#pragma unroll
  for (int t = 0; t < 4; ++t) { pl[t] = __shfl(p0, (lane & 48) | ((nf + t) & 15)); PIN(pl[t]); }
#pragma unroll 1
  for (int g = 0; 16 * g < nf; ++g) {
    const int back = G - 1 - g;
    const float p = back == 0 ? p0 : back == 1 ? p1 : p2;
#define PVB(BB) { const int R0 = 16 * g + 4 * (BB);                                             \
      if ((BB) == (WPA & 3)) WOASM(isA && g == (WPA >> 2) ? nt : 0);             \
      if ((BB) == (WPA & 3) && isA && g == (WPA >> 2)) { HEAD(); }           \
      if ((BB) == (WPB & 3) && !isA && g == (WPB >> 2)) { HEAD(); }        \
      if (R0 < nf) { u32x4 d[4]; FETCH4(R0, n, d)                                              \
        pv_fma(d[0], bcast16<4 * (BB)>(p), acc); pv_fma(d[1], bcast16<4 * (BB) + 1>(p), acc);   \
        pv_fma(d[2], bcast16<4 * (BB) + 2>(p), acc); pv_fma(d[3], bcast16<4 * (BB) + 3>(p), acc); } }
    PVB(0) PVB(1) PVB(2) PVB(3)
#undef PVB
  }
#pragma unroll
  for (int t = 0; t < 4; ++t) {
    if (nf + t < n) {
      const LAS u32x4* a_ = reinterpret_cast<const LAS u32x4*>(mine + wrapr(s0r + t) * ROWB);
      PIN(a_);
      for (unsigned sp = 0; F <= n + nf + t && sp < SPIN_CAP; ++sp) F = __builtin_amdgcn_readfirstlane(*full);
      asm volatile("" ::: "memory");
      pv_fma(*a_, pl[t], acc);
    }
  }
  {
    float t[4];
#pragma unroll
    for (int k = 0; k < 4; ++k) t[k] = fold32(acc[k], acc[k + 4]);
    *reinterpret_cast<LAS f32x2*>(la) = f32x2{fold16(t[0], t[2]) * f, fold16(t[1], t[3]) * f};
  }
  BAR();
  {
    f32x2 x = *reinterpret_cast<const LAS f32x2*>(lb);
#pragma unroll
    for (int v = 1; v < 4; ++v) x += *reinterpret_cast<const LAS f32x2*>(lb + v * APS);
    const float x0 = x[0] + dpp<XOR8>(x[0]), x1 = x[1] + dpp<XOR8>(x[1]);
    if (lane < 8) st_g16(po, x0, x1, tag);
  }
  WOASM(isA ? 0 : nt);
  u32x4 pt[3];
  if (nt) {
    {
      float e0, e1;
      poll(pg, tag, pt, e0, e1);
#pragma unroll
      for (int s = 0; s < 3; ++s) asm volatile("" : "+v"(wor[s][0]), "+v"(wor[s][1]), "+v"(wor[s][2]), "+v"(wor[s][3]));
      const bool on = (c & 7) < T;
      e0 = on ? e0 : 0.f; e1 = on ? e1 : 0.f;
      e0 += dpp<XOR1>(e0); e1 += dpp<XOR1>(e1);
      e0 += dpp<XOR2>(e0); e1 += dpp<XOR2>(e1);
      e0 += dpp<XOR7>(e0); e1 += dpp<XOR7>(e1);
      const float f0 = dpp<XOR8>(e0), f1 = dpp<XOR8>(e1);
      const float o[4] = {(c & 8) ? f0 : e0, (c & 8) ? f1 : e1, (c & 8) ? e0 : f0, (c & 8) ? e1 : f1};
      u32x4 af[4];
#pragma unroll
      for (int i = 0; i < 4; i += 2) {
        const unsigned hi = pack_bf16(o[i], o[i + 1]), lo = pack_bf16(o[i] - bf_lo(hi), o[i + 1] - bf_hi(hi));
        const unsigned pr = (c & 8) ? lo : hi;
#pragma unroll
        for (int a = 0; a < 4; ++a) { af[i][a] = __builtin_amdgcn_perm(0u, pr, sel[0][a]); af[i + 1][a] = __builtin_amdgcn_perm(0u, pr, sel[1][a]); }
      }
      auto tile = [&](int s) {
        f32x4 d = {0.f, 0.f, 0.f, 0.f};
#pragma unroll
        for (int i = 0; i < 4; ++i)
          d = __builtin_amdgcn_mfma_f32_16x16x32_bf16(__builtin_bit_cast(bf16x8, af[i]), __builtin_bit_cast(bf16x8, wor[s][i]), d, 0, 0, 0);
        *reinterpret_cast<LAS f32x2*>(yw + 128 * s) = f32x2{fold32(d[0], d[2]), fold32(d[1], d[3])};
      };
      tile(0);
      if (!isA) { tile(1); tile(2); }
    }
    BAR();
    if (ci < nt) {
      f32x2 y = *reinterpret_cast<const LAS f32x2*>(yp + 2 * ctid);
#pragma unroll
      for (int u = 1; u < NCONS; ++u) y += *reinterpret_cast<const LAS f32x2*>(yp + u * YPS + 2 * ctid);
      st_g16(py, y[0], y[1], tag);
    }
    poll_drain(pt);
  }
  if (red) {
    float s0, s1;
    poll(py, tag, pt, s0, s1);
    s0 += dpp<XOR2>(s0); s1 += dpp<XOR2>(s1);
    s0 += dpp<0x124>(s0); s1 += dpp<0x124>(s1);
    s0 += dpp<XOR8>(s0); s1 += dpp<XOR8>(s1);
    s0 = xrow_sum(s0); s1 = xrow_sum(s1);
    if (lane < 2) GPTR(unsigned, kc, 2)[2 * rb + lane] = pack_bf16(s0, s1);
  }
}

extern "C" void kernel_launch(void* const* d_in, const int* in_sizes, int n_in,
                              void* d_out, int out_size, void* d_ws, size_t ws_size,
                              hipStream_t stream) {
  typedef const __hip_bfloat16* P;
  attn_main<<<NWG, NTHR, 0, stream>>>((P)d_in[0], (P)d_in[1], (P)d_in[2], (P)d_in[3], (P)d_in[4],
                                      (P)d_in[5], (P)d_in[6], (P)d_in[7], (P)d_in[8], (P)d_in[9],
                                      (u32x4*)d_ws, (__hip_bfloat16*)d_out);
}
```
